```python
import torch

def _softmax(x, axis=-1):
    return torch.softmax(x, dim=axis)
def _gelu(x):
    return torch.nn.functional.gelu(x, approximate="tanh")
def _silu(x):
    return torch.nn.functional.silu(x)
def _sigmoid(x):
    return torch.sigmoid(x)
def _layernorm(x, axis=-1, eps=1e-5):
    m = x.mean(dim=axis, keepdim=True)
    v = x.var(dim=axis, keepdim=True, unbiased=False)
    return (x - m) / torch.sqrt(v + eps)
def _scatter_add(tgt, idx, src, axis=0):
    return tgt.clone().index_add_(axis, idx, src)
def _segment_sum(src, seg, n):
    out = torch.zeros((n,) + src.shape[1:], dtype=src.dtype)
    return out.index_add_(0, seg, src)
def _pad(x, pw):
    flat = tuple(v for pair in reversed(pw) for v in pair)
    return torch.nn.functional.pad(x, flat)

kernel_name = 'concat_heads__gqa_block_cached__gqa_block_cached__near_ridge'

def setup_inputs():

    _w = (lambda a: (lambda t: t.to(torch.bfloat16) if t.is_floating_point() else t)(torch.as_tensor(a)))
    def _A_setup():
        B, Sq, D = 16, 96, 512
        Hq, Hkv, Sk, Dh = 32, 32, 512, 128
        assert Hq % Hkv == 0, (Hq, Hkv)
        return {
            "x": _w((lambda *s: torch.randn(*s, dtype=torch.bfloat16) * 0.8)(B, Sq, D)),
            "k_cache": _w((lambda *s: torch.randn(*s, dtype=torch.bfloat16) * 0.8)(B, Hkv, Sk, Dh)),
            "v_cache": _w((lambda *s: torch.randn(*s, dtype=torch.bfloat16) * 0.05)(B, Hkv, Sk, Dh)),
            "wq": _w((lambda *s: torch.randn(*s, dtype=torch.bfloat16) * 0.8)(D, Hq * Dh) * (D ** -0.5)),
            "wo": _w((lambda *s: torch.randn(*s, dtype=torch.bfloat16) * 0.05)(Hq * Dh, D) * ((Hq * Dh) ** -0.5)),
        }
    _A_in = _A_setup()

    def _B_setup():
        B, Sq, D = 16, 96, 512
        Hq, Hkv, Sk, Dh = 32, 32, 512, 128
        assert Hq % Hkv == 0, (Hq, Hkv)
        return {
            "x": _w((lambda *s: torch.randn(*s, dtype=torch.bfloat16) * 0.8)(B, Sq, D)),
            "k_cache": _w((lambda *s: torch.randn(*s, dtype=torch.bfloat16) * 0.8)(B, Hkv, Sk, Dh)),
            "v_cache": _w((lambda *s: torch.randn(*s, dtype=torch.bfloat16) * 0.05)(B, Hkv, Sk, Dh)),
            "wq": _w((lambda *s: torch.randn(*s, dtype=torch.bfloat16) * 0.8)(D, Hq * Dh) * (D ** -0.5)),
            "wo": _w((lambda *s: torch.randn(*s, dtype=torch.bfloat16) * 0.05)(Hq * Dh, D) * ((Hq * Dh) ** -0.5)),
        }
    _B_in = _B_setup()

    return {
        "A_x": _A_in["x"],
        "A_k_cache": _A_in["k_cache"],
        "A_v_cache": _A_in["v_cache"],
        "A_wq": _A_in["wq"],
        "A_wo": _A_in["wo"],
        "B_x": _B_in["x"],
        "B_k_cache": _B_in["k_cache"],
        "B_v_cache": _B_in["v_cache"],
        "B_wq": _B_in["wq"],
        "B_wo": _B_in["wo"],
    }

def reference(A_x, A_k_cache, A_v_cache, A_wq, A_wo, B_x, B_k_cache, B_v_cache, B_wq, B_wo):
    def _A_ref(x, k_cache, v_cache, wq, wo):
        B, Sq, D = x.shape
        Hq, Dh = 32, 128
        Hkv = k_cache.shape[1]
        rep = Hq // Hkv
        q = (lambda x, *d: x.permute(*d))((x @ wq).reshape(B, Sq, Hq, Dh), 0, 2, 1, 3)
        k = (lambda x, n, axis: torch.repeat_interleave(x, n, dim=axis))(k_cache, rep, axis=1)
        v = (lambda x, n, axis: torch.repeat_interleave(x, n, dim=axis))(v_cache, rep, axis=1)
        scores = torch.einsum("bhqd,bhkd->bhqk", q, k) * (Dh ** -0.5)
        p = _softmax(scores, axis=-1)
        o = torch.einsum("bhqk,bhkd->bhqd", p, v)
        return (lambda x, *d: x.permute(*d))(o, 0, 2, 1, 3).reshape(B, Sq, Hq * Dh) @ wo

    def _B_ref(x, k_cache, v_cache, wq, wo):
        B, Sq, D = x.shape
        Hq, Dh = 32, 128
        Hkv = k_cache.shape[1]
        rep = Hq // Hkv
        q = (lambda x, *d: x.permute(*d))((x @ wq).reshape(B, Sq, Hq, Dh), 0, 2, 1, 3)
        k = (lambda x, n, axis: torch.repeat_interleave(x, n, dim=axis))(k_cache, rep, axis=1)
        v = (lambda x, n, axis: torch.repeat_interleave(x, n, dim=axis))(v_cache, rep, axis=1)
        scores = torch.einsum("bhqd,bhkd->bhqk", q, k) * (Dh ** -0.5)
        p = _softmax(scores, axis=-1)
        o = torch.einsum("bhqk,bhkd->bhqd", p, v)
        return (lambda x, *d: x.permute(*d))(o, 0, 2, 1, 3).reshape(B, Sq, Hq * Dh) @ wo

    a = _A_ref(A_x, A_k_cache, A_v_cache, A_wq, A_wo)
    b = _B_ref(B_x, B_k_cache, B_v_cache, B_wq, B_wo)
    return torch.concatenate([a, b], axis=-1)
```

```cpp
#include <hip/hip_runtime.h>
#include <hip/hip_bf16.h>
#include <stdint.h>

typedef unsigned short u16;
__device__ __forceinline__ float bf2f(u16 v) { return __uint_as_float(((unsigned)v) << 16); }
__device__ __forceinline__ u16 f2bf(float f) { unsigned u = __float_as_uint(f); u += 0x7fffu + ((u >> 16) & 1u); return (u16)(u >> 16); }

constexpr int B = 16, SQ = 96, D = 512, H = 32, SK = 512, DH = 128;

__global__ void naive_q(const u16* x, const u16* wq, u16* q) {
  int n = blockIdx.x * 256 + threadIdx.x;
  int row = blockIdx.y;
  float acc = 0.f;
  for (int c = 0; c < D; ++c) acc += bf2f(x[row * D + c]) * bf2f(wq[(size_t)c * 4096 + n]);
  q[(size_t)row * 4096 + n] = f2bf(acc);
}
__global__ void naive_attn(const u16* q, const u16* k, const u16* v, u16* o) {
  __shared__ float qs[DH]; __shared__ float p[SK]; __shared__ float red[512];
  int s = blockIdx.x, h = blockIdx.y, b = blockIdx.z, t = threadIdx.x;
  if (t < DH) qs[t] = bf2f(q[((size_t)(b * SQ + s)) * 4096 + h * DH + t]);
  __syncthreads();
  const u16* kr = k + (((size_t)b * H + h) * SK + t) * DH;
  float acc = 0.f;
  for (int d = 0; d < DH; ++d) acc += qs[d] * bf2f(kr[d]);
  float sc = bf2f(f2bf(bf2f(f2bf(acc)) * bf2f(f2bf(0.08838834764831845f))));
  red[t] = sc; __syncthreads();
  for (int st = 256; st > 0; st >>= 1) { if (t < st) red[t] = fmaxf(red[t], red[t + st]); __syncthreads(); }
  float m = red[0]; __syncthreads();
  float e = expf(sc - m);
  red[t] = e; __syncthreads();
  for (int st = 256; st > 0; st >>= 1) { if (t < st) red[t] += red[t + st]; __syncthreads(); }
  float l = red[0];
  p[t] = bf2f(f2bf(e / l));
  __syncthreads();
  if (t < DH) {
    float a = 0.f;
    const u16* vb = v + (((size_t)b * H + h) * SK) * DH + t;
    for (int j = 0; j < SK; ++j) a += p[j] * bf2f(vb[(size_t)j * DH]);
    o[((size_t)(b * SQ + s)) * 4096 + h * DH + t] = f2bf(a);
  }
}
__global__ void naive_out(const u16* o, const u16* wo, u16* out, int coloff) {
  int n = blockIdx.x * 256 + threadIdx.x;
  int row = blockIdx.y;
  float acc = 0.f;
  for (int c = 0; c < 4096; ++c) acc += bf2f(o[(size_t)row * 4096 + c]) * bf2f(wo[(size_t)c * 512 + n]);
  out[(size_t)row * 1024 + coloff + n] = f2bf(acc);
}

extern "C" void kernel_launch(void* const* d_in, const int* in_sizes, int n_in, void* d_out, int out_size, void* d_ws, size_t ws_size, hipStream_t stream) {
  u16* q = (u16*)d_ws; u16* o = q + (size_t)1536 * 4096;
  for (int br = 0; br < 2; ++br) {
    const u16* x = (const u16*)d_in[5 * br + 0]; const u16* k = (const u16*)d_in[5 * br + 1]; const u16* v = (const u16*)d_in[5 * br + 2];
    const u16* wq = (const u16*)d_in[5 * br + 3]; const u16* wo = (const u16*)d_in[5 * br + 4];
    naive_q<<<dim3(16, 1536), 256, 0, stream>>>(x, wq, q);
    naive_attn<<<dim3(SQ, H, B), 512, 0, stream>>>(q, k, v, o);
    naive_out<<<dim3(2, 1536), 256, 0, stream>>>(o, wo, (u16*)d_out, br * 512);
  }
}
```

```cpp
#include <hip/hip_runtime.h>
#include <stdint.h>

typedef unsigned short u16;
typedef short bf16x8 __attribute__((ext_vector_type(8)));
typedef short s16x4 __attribute__((ext_vector_type(4)));
typedef float f32x16 __attribute__((ext_vector_type(16)));
typedef float f32x2 __attribute__((ext_vector_type(2)));
typedef __bf16 bf16x2 __attribute__((ext_vector_type(2)));
typedef unsigned u32x4 __attribute__((ext_vector_type(4)));
#define LAS __attribute__((address_space(3)))
#define DI __device__ __forceinline__
typedef LAS const char* lds_cptr;
typedef LAS char* lds_ptr;

constexpr int QO_OFF = 0;
constexpr int WO_OFF = 24576, WO_SLOT = 8192;
constexpr int KV_OFF = 49152, KV_SLOT = 16384, KV_END = 163840;
constexpr int KV_FIRST = KV_OFF + 5 * KV_SLOT;
constexpr int WS_A = 0, WS_C = 49152, WS_B = 88064;
constexpr int WS_X = 32768;
constexpr int LDS_BYTES = 163840;
constexpr float C_SCALE = 0.08838834764831845f * 1.4426950408889634f;
constexpr float THR = 6.0f;

#define QKS 2
#define MFMA(a, b, c) __builtin_amdgcn_mfma_f32_32x32x16_bf16((a), (b), (c), 0, 0, 0)

DI unsigned cvtpk(float lo, float hi) { f32x2 v = {lo, hi}; bf16x2 b = __builtin_convertvector(v, bf16x2); return __builtin_bit_cast(unsigned, b); }
DI bf16x8 ld128(lds_cptr p) { return *(const LAS bf16x8*)p; }
DI void st128(lds_ptr p, u32x4 v) { *(LAS u32x4*)p = v; }
DI s16x4 ldtr(lds_cptr p) { typedef short v4i16_t __attribute__((ext_vector_type(4))); return __builtin_bit_cast(s16x4, __builtin_amdgcn_ds_read_tr16_b64_v4i16((LAS v4i16_t*)p)); }
DI bf16x8 cat(s16x4 lo, s16x4 hi) { return __builtin_shufflevector(lo, hi, 0, 1, 2, 3, 4, 5, 6, 7); }
DI u32x4 pack8s(const f32x16& x, int s, float sc) {
  u32x4 w = {cvtpk(x[8 * s] * sc, x[8 * s + 1] * sc), cvtpk(x[8 * s + 2] * sc, x[8 * s + 3] * sc), cvtpk(x[8 * s + 4] * sc, x[8 * s + 5] * sc), cvtpk(x[8 * s + 6] * sc, x[8 * s + 7] * sc)};
  return w;
}
DI u32x4 pack8n(const f32x16& x, int s) {
  u32x4 w = {cvtpk(x[8 * s], x[8 * s + 1]), cvtpk(x[8 * s + 2], x[8 * s + 3]), cvtpk(x[8 * s + 4], x[8 * s + 5]), cvtpk(x[8 * s + 6], x[8 * s + 7])};
  return w;
}
DI bf16x8 asfrag(u32x4 v) { return __builtin_bit_cast(bf16x8, v); }
DI void dma16(__amdgpu_buffer_rsrc_t srd, unsigned voff, unsigned soff, unsigned ldsaddr) {
  asm volatile("s_mov_b32 m0, %0\n\ts_nop 3\n\tbuffer_load_dwordx4 %1, %2, %3 offen lds" ::"s"(ldsaddr), "v"(voff), "s"(srd), "s"(soff) : "memory");
}
DI void dma16nt(__amdgpu_buffer_rsrc_t srd, unsigned voff, unsigned soff, unsigned ldsaddr) {
  asm volatile("s_mov_b32 m0, %0\n\ts_nop 3\n\tbuffer_load_dwordx4 %1, %2, %3 offen nt lds" ::"s"(ldsaddr), "v"(voff), "s"(srd), "s"(soff) : "memory");
}
#define WAITVM(n) asm volatile("s_waitcnt vmcnt(" #n ")" ::: "memory")

#define STAMP(i)
#define BAR() asm volatile("s_waitcnt lgkmcnt(0)\n\ts_barrier" ::: "memory")

__global__ __launch_bounds__(512, 2) void fused_attn(const u16* __restrict__ xA, const u16* __restrict__ kA, const u16* __restrict__ vA, const u16* __restrict__ wqA,
                                                   const u16* __restrict__ woA, const u16* __restrict__ xB, const u16* __restrict__ kB, const u16* __restrict__ vB,
                                                   const u16* __restrict__ wqB, const u16* __restrict__ woB, u16* __restrict__ slab
) {
  extern __shared__ __attribute__((aligned(16))) char smem[];
  const lds_ptr lds = (lds_ptr)smem;
  const unsigned lds0 = (unsigned)(uintptr_t)smem;
  const int tid = threadIdx.x, lane = tid & 63;
  const int w = __builtin_amdgcn_readfirstlane(tid >> 6);
  const int bid = blockIdx.x, xcd = bid & 7, idx = bid >> 3;
  const int br = xcd >> 2, g = 2 * (xcd & 3) + (idx & 1), b = idx >> 1;
  const int l32 = lane & 31, h = lane >> 5;
  const int blk = (lane >> 4) & 1, q4 = (lane & 15) >> 2, p = lane & 3;
  const unsigned r8 = (lane >> 2) & 7, slot4 = lane & 3;
  const unsigned wqb = 2048u * h + 64u * q4 + 32u * blk + 16u * (p & 1) + 8u * (p >> 1);
  const unsigned xb0 = WS_X + 512u * (l32 >> 3) + 64u * (l32 & 7) + 16u * (h ^ ((l32 >> 2) & 3));

  if (w < 3) {
    const int qb = w;
    const u16* xg = br ? xB : xA;
    if (w == 0) STAMP(0);
    {
      const __amdgpu_buffer_rsrc_t srdX = __builtin_amdgcn_make_buffer_rsrc((void*)(xg + (size_t)b * 96 * 512), 0, 96 * 512 * 2, 0x00020000);
      unsigned d0, d1;
      const unsigned vo = ((g & 1) * 384u + qb * 128u + lane) * 128u;
      asm volatile("buffer_load_dword %0, %2, %4, 0 offen\n\tbuffer_load_dword %1, %3, %4, 0 offen\n\ts_waitcnt vmcnt(0)"
                   : "=&v"(d0), "=&v"(d1) : "v"(vo), "v"(vo + 8192u), "s"(srdX) : "memory");
    }
    bf16x8 qf[8], qf1[8];
    {
      f32x16 acc[8];
#pragma unroll
      for (int d = 0; d < 8; ++d) acc[d] = f32x16{};
      BAR();
      int st = WS_A;
      for (int j = 0; j < 16; ++j) {
        const lds_cptr wp = lds + st + wqb;
        const lds_cptr xp0 = lds + st + xb0 + 2048 * qb, xp1 = lds + st + (xb0 ^ 32u) + 2048 * qb;
#pragma unroll
        for (int ks = 0; ks < QKS; ++ks) {
          const bf16x8 xf = ld128(ks ? xp1 : xp0);
#pragma unroll
          for (int d = 0; d < 8; ++d)
            acc[d] = MFMA(cat(ldtr(wp + 8192 * (d >> 2) + 4096 * ks + 512 * (d & 3)), ldtr(wp + 8192 * (d >> 2) + 4096 * ks + 256 + 512 * (d & 3))), xf, acc[d]);
        }
        st = (st == WS_A) ? WS_C : (st == WS_C) ? WS_B : WS_A;
        BAR();
      }
#pragma unroll
      for (int d = 0; d < 4; ++d) {
        qf[2 * d] = asfrag(pack8n(acc[d], 0)); qf[2 * d + 1] = asfrag(pack8n(acc[d], 1));
        qf1[2 * d] = asfrag(pack8n(acc[4 + d], 0)); qf1[2 * d + 1] = asfrag(pack8n(acc[4 + d], 1));
      }
    }
    if (w == 0) STAMP(1);
    const unsigned kb0 = 2048u * (l32 >> 3) + 64u * (l32 & 7) + 16u * (h ^ ((l32 >> 2) & 3));
    const unsigned vb0 = 8192u + 256u * h + 64u * q4 + 32u * blk + 8u * p;
    const lds_ptr qo = lds + QO_OFF + qb * 8192 + lane * 16;
    f32x16 o[4];
    float mref = 0.f, lsum = 0.f;
    int kvoff = KV_FIRST;
    for (int u = 0; u <= 4; ++u) {
      if (u == 1) {
#pragma unroll
        for (int s = 0; s < 8; ++s) qf[s] = qf1[s];
      } else if (u == 2 || u == 3) {
#pragma unroll
        for (int s = 0; s < 8; ++s) qf[s] = ld128(qo + s * 1024);
      }
      if (u > 0) {
        auto rr = __builtin_amdgcn_permlane32_swap(__float_as_uint(lsum), __float_as_uint(lsum), false, false);
        const float inv = 1.0f / (__uint_as_float(rr[0]) + __uint_as_float(rr[1]));
        asm volatile("s_waitcnt lgkmcnt(0)" ::: "memory");
#pragma unroll
        for (int dt = 0; dt < 4; ++dt) {
          st128(qo + (2 * dt) * 1024, pack8s(o[dt], 0, inv));
          st128(qo + (2 * dt + 1) * 1024, pack8s(o[dt], 1, inv));
        }
      }
      if (u == 4) { BAR(); break; }
#pragma unroll
      for (int dt = 0; dt < 4; ++dt) o[dt] = f32x16{};
      mref = -1e30f; lsum = 0.f;
      for (int t = 0; t < 16; ++t) {
        const lds_cptr kv = lds + kvoff;
        const lds_cptr kp0 = kv + kb0, kp1 = kv + (kb0 ^ 32u), vp = kv + vb0;
        f32x16 sc = f32x16{};
#pragma unroll
        for (int s = 0; s < 8; ++s) sc = MFMA(ld128(((s & 1) ? kp1 : kp0) + 512 * (s >> 1)), qf[s], sc);
        bf16x8 vf[4][2];
#pragma unroll
        for (int ks = 0; ks < 2; ++ks)
#pragma unroll
          for (int dt = 0; dt < 4; ++dt) vf[dt][ks] = cat(ldtr(vp + 4096 * ks + 512 * dt), ldtr(vp + 4096 * ks + 2048 + 512 * dt));
        float pm = fmaxf(fmaxf(sc[0], sc[1]), sc[2]);
#pragma unroll
        for (int r = 3; r < 15; r += 2) pm = fmaxf(fmaxf(pm, sc[r]), sc[r + 1]);
        pm = fmaxf(pm, sc[15]);
        { auto rr = __builtin_amdgcn_permlane32_swap(__float_as_uint(pm), __float_as_uint(pm), false, false);
          pm = fmaxf(__uint_as_float(rr[0]), __uint_as_float(rr[1])) * C_SCALE; }
        if (__builtin_expect(__any(pm - mref > THR), 0)) {
          const float mn = fmaxf(mref, pm);
          const float alpha = __builtin_amdgcn_exp2f(mref - mn);
          mref = mn; lsum *= alpha;
#pragma unroll
          for (int dt = 0; dt < 4; ++dt)
#pragma unroll
            for (int r = 0; r < 16; ++r) o[dt][r] *= alpha;
        }
        const float nm = -mref;
        float ps = 0.f;
#pragma unroll
        for (int r = 0; r < 16; ++r) { sc[r] = __builtin_amdgcn_exp2f(__builtin_fmaf(sc[r], C_SCALE, nm)); ps += sc[r]; }
        lsum += ps;
        const bf16x8 pf0 = asfrag(pack8n(sc, 0)), pf1 = asfrag(pack8n(sc, 1));
#pragma unroll
        for (int dt = 0; dt < 4; ++dt) o[dt] = MFMA(vf[dt][0], pf0, o[dt]);
#pragma unroll
        for (int dt = 0; dt < 4; ++dt) o[dt] = MFMA(vf[dt][1], pf1, o[dt]);
        kvoff = (kvoff == KV_END - KV_SLOT) ? KV_OFF : kvoff + KV_SLOT;
        BAR();
      }
      if (w == 0) STAMP(2 + u);
    }
  } else if (w == 3) {
    const u16* kg = br ? kB : kA; const u16* vg = br ? vB : vA; const u16* wog = br ? woB : woA;
    const size_t kvbase = ((size_t)b * 32 + 4 * g) * (512 * 128);
    const __amdgpu_buffer_rsrc_t srdK = __builtin_amdgcn_make_buffer_rsrc((void*)(kg + kvbase), 0, 4 * 512 * 128 * 2, 0x00020000);
    const __amdgpu_buffer_rsrc_t srdV = __builtin_amdgcn_make_buffer_rsrc((void*)(vg + kvbase), 0, 4 * 512 * 128 * 2, 0x00020000);
    const unsigned voffV = r8 * 256u + 64u * h + 16u * slot4;
    const unsigned voffK0 = r8 * 256u + 64u * h + 16u * (slot4 ^ (r8 >> 2)), voffK1 = voffK0 ^ 32u;
    auto issue_kv = [&](int T, int slot) {
      const unsigned so = (unsigned)T * 8192u, dst = lds0 + slot;
#pragma unroll
      for (int i = 0; i < 8; ++i) dma16nt(srdK, ((i >> 1) & 1) ? voffK1 : voffK0, so + 2048u * (i >> 1) + 128u * (i & 1), dst + 1024u * i);
#pragma unroll
      for (int i = 0; i < 8; ++i) dma16nt(srdV, voffV, so + 2048u * (i >> 1) + 128u * (i & 1), dst + 8192u + 1024u * i);
    };
    issue_kv(0, KV_FIRST);
    issue_kv(1, KV_FIRST + KV_SLOT);
    bf16x8 q2[2][3][8];
    {
      f32x16 acc[8];
#pragma unroll
      for (int d = 0; d < 8; ++d) acc[d] = f32x16{};
      BAR();
      int st = WS_A;
      for (int j = 0; j < 16; ++j) {
        const lds_cptr wp = lds + st + wqb + 16384;
        const lds_cptr xp0 = lds + st + xb0 + 4096, xp1 = lds + st + (xb0 ^ 32u) + 4096;
#pragma unroll
        for (int ks = 0; ks < QKS; ++ks) {
          const bf16x8 xf = ld128(ks ? xp1 : xp0);
#pragma unroll
          for (int d = 0; d < 8; ++d)
            acc[d] = MFMA(cat(ldtr(wp + 8192 * (d >> 2) + 4096 * ks + 512 * (d & 3)), ldtr(wp + 8192 * (d >> 2) + 4096 * ks + 256 + 512 * (d & 3))), xf, acc[d]);
        }
        st = (st == WS_A) ? WS_C : (st == WS_C) ? WS_B : WS_A;
        if (j == 14) { issue_kv(2, KV_OFF); issue_kv(3, KV_OFF + KV_SLOT); }
        if (j == 15) { issue_kv(4, KV_OFF + 2 * KV_SLOT); WAITVM(48); }
        BAR();
      }
#pragma unroll
      for (int d = 0; d < 8; ++d) { q2[d >> 2][2][2 * (d & 3)] = asfrag(pack8n(acc[d], 0)); q2[d >> 2][2][2 * (d & 3) + 1] = asfrag(pack8n(acc[d], 1)); }
    }
    issue_kv(5, KV_OFF + 3 * KV_SLOT); issue_kv(6, KV_OFF + 4 * KV_SLOT);
    WAITVM(48);
    BAR();
    {
      const lds_cptr hp = lds + lane * 16;
#pragma unroll
      for (int d = 0; d < 8; ++d)
#pragma unroll
        for (int qb = 0; qb < 2; ++qb)
#pragma unroll
          for (int s = 0; s < 2; ++s) q2[d >> 2][qb][2 * (d & 3) + s] = ld128(hp + ((d * 2 + qb) * 2 + s) * 1024);
    }
    int islot = KV_FIRST;
    const lds_ptr qo = lds + QO_OFF + lane * 16;
    const unsigned voffWo = r8 * 1024u + 64u * h + 16u * slot4;
    const __amdgpu_buffer_rsrc_t srdWo = __builtin_amdgcn_make_buffer_rsrc((void*)(wog + (size_t)g * 512 * 512), 0, 512 * 512 * 2, 0x00020000);
    for (int S = 17; S < 74; ++S) {
      issue_kv(S - 10, islot);
      islot = (islot == KV_END - KV_SLOT) ? KV_OFF : islot + KV_SLOT;
      if (S == 47) {
#pragma unroll
        for (int qb = 0; qb < 3; ++qb)
#pragma unroll
          for (int s = 0; s < 8; ++s) st128(qo + qb * 8192 + s * 1024, __builtin_bit_cast(u32x4, q2[0][qb][s]));
      }
      if (S == 63) {
#pragma unroll
        for (int qb = 0; qb < 3; ++qb)
#pragma unroll
          for (int s = 0; s < 8; ++s) st128(qo + qb * 8192 + s * 1024, __builtin_bit_cast(u32x4, q2[1][qb][s]));
      }
      WAITVM(48);
      BAR();
    }
    for (int r = 0; r < 7; ++r) {
      const unsigned rr = (r + (b >> 1)) & 7;
      const unsigned so = (384u + 16u * rr) * 1024u, dst = lds0 + islot;
#pragma unroll
      for (int i = 0; i < 16; ++i)
        dma16(srdWo, voffWo, so + 512u * (i >> 3) + 8192u * ((i >> 2) & 1) + 128u * (i & 3), dst + 1024u * i);
      islot = (islot == KV_END - KV_SLOT) ? KV_OFF : islot + KV_SLOT;
      if (r < 6) WAITVM(48); else WAITVM(0);
      BAR();
    }
  } else {
    const int pw = w - 4;
    const u16* xg = br ? xB : xA; const u16* wqg = br ? wqB : wqA; const u16* wog = br ? woB : woA;
    {
      const __amdgpu_buffer_rsrc_t srdWq = __builtin_amdgcn_make_buffer_rsrc((void*)(wqg + g * 512), 0, 512 * 4096 * 2 - g * 1024, 0x00020000);
      const __amdgpu_buffer_rsrc_t srdX = __builtin_amdgcn_make_buffer_rsrc((void*)(xg + (size_t)b * 96 * 512), 0, 96 * 512 * 2, 0x00020000);
      const unsigned voffWq = r8 * 8192u + 64u * h + 16u * slot4;
      const unsigned voffX = (8u * h + r8) * 1024u + 16u * (slot4 ^ (2u * h + (r8 >> 2)));
      auto issue_w = [&](int j, int st) {
        const unsigned jj = (j + b) & 15;
        const unsigned so = jj * (32u * 8192u) + pw * 256u, dst = lds0 + st;
#pragma unroll
        for (int i = 0; i < 8; ++i) dma16(srdWq, voffWq, so + 65536u * (i >> 1) + 128u * (i & 1), dst + 8192u * pw + 1024u * i);
        if (pw < 2) {
          dma16(srdX, voffX, jj * 64u + (2u * pw) * 16384u, dst + WS_X + (2u * pw) * 1024u);
          dma16(srdX, voffX, jj * 64u + (2u * pw + 1) * 16384u, dst + WS_X + (2u * pw + 1) * 1024u);
        } else {
          dma16(srdX, voffX, jj * 64u + (2u + pw) * 16384u, dst + WS_X + (2u + pw) * 1024u);
        }
      };
      issue_w(0, WS_A);
      issue_w(1, WS_C);
      f32x16 acc[2][2];
#pragma unroll
      for (int d = 0; d < 2; ++d) { acc[d][0] = f32x16{}; acc[d][1] = f32x16{}; }
      if (pw < 2) WAITVM(10); else WAITVM(9);
      BAR();
      int st = WS_A, st2 = WS_B;
      for (int j = 0; j < 16; ++j) {
        if (j < 14) issue_w(j + 2, st2);
        const lds_cptr wp = lds + st + wqb + 16384 + 8192 * (pw >> 1) + 1024 * (pw & 1);
        const lds_cptr xp0 = lds + st + xb0, xp1 = lds + st + (xb0 ^ 32u);
#pragma unroll
        for (int ks = 0; ks < QKS; ++ks) {
          const bf16x8 xf0 = ld128(ks ? xp1 : xp0), xf1 = ld128((ks ? xp1 : xp0) + 2048);
#pragma unroll
          for (int d = 0; d < 2; ++d) {
            const bf16x8 wf = cat(ldtr(wp + 4096 * ks + 512 * d), ldtr(wp + 4096 * ks + 256 + 512 * d));
            acc[d][0] = MFMA(wf, xf0, acc[d][0]);
            acc[d][1] = MFMA(wf, xf1, acc[d][1]);
          }
        }
        st = (st == WS_A) ? WS_C : (st == WS_C) ? WS_B : WS_A;
        st2 = (st2 == WS_A) ? WS_C : (st2 == WS_C) ? WS_B : WS_A;
        if (j < 14) { if (pw < 2) WAITVM(10); else WAITVM(9); } else WAITVM(0);
        BAR();
      }
      const lds_ptr hp = lds + lane * 16;
#pragma unroll
      for (int d = 0; d < 2; ++d)
#pragma unroll
        for (int qb = 0; qb < 2; ++qb)
#pragma unroll
          for (int s = 0; s < 2; ++s) st128(hp + (((2 * pw + d) * 2 + qb) * 2 + s) * 1024, pack8n(acc[d][qb], s));
      BAR();
    }
    for (int S = 17; S < 31; ++S) BAR();
    const __amdgpu_buffer_rsrc_t srdWo = __builtin_amdgcn_make_buffer_rsrc((void*)(wog + (size_t)g * 512 * 512), 0, 512 * 512 * 2, 0x00020000);
    const unsigned voffWo = (8u * (pw >> 1) + r8) * 1024u + 64u * (4u * (pw & 1) + h) + 16u * slot4;
    auto issue_wo = [&](int u, int m, int slot) {
      const unsigned rr = ((m >> 1) + (b >> 1)) & 7;
      const unsigned so = (128u * u + 16u * rr) * 1024u + ((m ^ b) & 1) * 512u;
      const unsigned dst = lds0 + WO_OFF + slot + 2048u * pw;
      dma16(srdWo, voffWo, so, dst);
      dma16(srdWo, voffWo, so + 128u, dst + 1024u);
    };
    const unsigned ob0 = 256u * h + 64u * q4 + 32u * blk + 16u * (p & 1) + 8u * (p >> 1) + 1024u * pw;
    const lds_cptr qo = lds + QO_OFF + lane * 16;
    f32x16 acc[2][2][3];
#pragma unroll
    for (int a = 0; a < 2; ++a)
#pragma unroll
      for (int c = 0; c < 2; ++c)
#pragma unroll
        for (int qb = 0; qb < 3; ++qb) acc[a][c][qb] = f32x16{};
    bf16x8 of[3], ofn[3];
    issue_wo(0, 0, 0 * WO_SLOT);  BAR();
    issue_wo(0, 1, 1 * WO_SLOT);  WAITVM(2); BAR();
    int cur = 0, nxt2 = 2 * WO_SLOT;
    auto rot = [&]() { cur = (cur == 2 * WO_SLOT) ? 0 : cur + WO_SLOT; nxt2 = (nxt2 == 2 * WO_SLOT) ? 0 : nxt2 + WO_SLOT; };
    const int rb = b >> 1;
    for (int M = 0; M < 48; M += 2) {
      const int m = M & 15;
      {
        if (M + 2 < 48) issue_wo((M + 2) >> 4, (M + 2) & 15, nxt2); else issue_wo(3, 14, nxt2);
        if (m == 0) {
#pragma unroll
          for (int qb = 0; qb < 3; ++qb) of[qb] = ld128(qo + qb * 8192 + (rb & 7) * 1024);
        }
        const lds_cptr wp = lds + WO_OFF + cur + ob0;
#pragma unroll
        for (int c = 0; c < 2; ++c) {
          const bf16x8 wf = cat(ldtr(wp + 512 * c), ldtr(wp + 4096 + 512 * c));
#pragma unroll
          for (int qb = 0; qb < 3; ++qb) acc[0][c][qb] = MFMA(wf, of[qb], acc[0][c][qb]);
        }
        WAITVM(2);
        BAR();
        rot();
      }
      {
        if (M + 3 < 48) issue_wo((M + 3) >> 4, (M + 3) & 15, nxt2); else issue_wo(3, 15, nxt2);
        if (m != 14) {
#pragma unroll
          for (int qb = 0; qb < 3; ++qb) ofn[qb] = ld128(qo + qb * 8192 + (((m >> 1) + 1 + rb) & 7) * 1024);
        }
        const lds_cptr wp = lds + WO_OFF + cur + ob0;
#pragma unroll
        for (int c = 0; c < 2; ++c) {
          const bf16x8 wf = cat(ldtr(wp + 512 * c), ldtr(wp + 4096 + 512 * c));
#pragma unroll
          for (int qb = 0; qb < 3; ++qb) acc[1][c][qb] = MFMA(wf, of[qb], acc[1][c][qb]);
        }
#pragma unroll
        for (int qb = 0; qb < 3; ++qb) of[qb] = ofn[qb];
        if (M + 3 < 48) WAITVM(2); else WAITVM(0);
        BAR();
        rot();
      }
    }
    if (pw == 0) STAMP(7);
    {
      int sl = KV_OFF + ((74 - 17 + 5) % 7) * KV_SLOT;
      const unsigned hsel = (b & 1) * 8192u;
      for (int r = 0; r < 7; ++r) {
#pragma unroll
        for (int qb = 0; qb < 3; ++qb) of[qb] = ld128(qo + qb * 8192 + ((r + rb) & 7) * 1024);
        const lds_cptr wp0 = lds + sl + ob0 + hsel, wp1 = lds + sl + ob0 + (hsel ^ 8192u);
#pragma unroll
        for (int c = 0; c < 2; ++c) {
          const bf16x8 wf0 = cat(ldtr(wp0 + 512 * c), ldtr(wp0 + 4096 + 512 * c));
          const bf16x8 wf1 = cat(ldtr(wp1 + 512 * c), ldtr(wp1 + 4096 + 512 * c));
#pragma unroll
          for (int qb = 0; qb < 3; ++qb) { acc[0][c][qb] = MFMA(wf0, of[qb], acc[0][c][qb]); acc[1][c][qb] = MFMA(wf1, of[qb], acc[1][c][qb]); }
        }
        sl = (sl == KV_END - KV_SLOT) ? KV_OFF : sl + KV_SLOT;
      }
#pragma unroll
      for (int qb = 0; qb < 3; ++qb) of[qb] = ld128(qo + qb * 8192 + ((7 + rb) & 7) * 1024);
      const lds_cptr wp0 = lds + WO_OFF + cur + ob0;
      const int cur1 = (cur == 2 * WO_SLOT) ? 0 : cur + WO_SLOT;
      const lds_cptr wp1 = lds + WO_OFF + cur1 + ob0;
#pragma unroll
      for (int c = 0; c < 2; ++c) {
        const bf16x8 wf0 = cat(ldtr(wp0 + 512 * c), ldtr(wp0 + 4096 + 512 * c));
        const bf16x8 wf1 = cat(ldtr(wp1 + 512 * c), ldtr(wp1 + 4096 + 512 * c));
#pragma unroll
        for (int qb = 0; qb < 3; ++qb) { acc[0][c][qb] = MFMA(wf0, of[qb], acc[0][c][qb]); acc[1][c][qb] = MFMA(wf1, of[qb], acc[1][c][qb]); }
      }
    }
    char* sl = (char*)slab + (size_t)((br * 16 + b) * 8 + g) * 98304 + lane * 16;
#pragma unroll
    for (int a = 0; a < 2; ++a)
#pragma unroll
      for (int qb = 0; qb < 3; ++qb)
#pragma unroll
        for (int c = 0; c < 2; ++c)
#pragma unroll
          for (int s = 0; s < 2; ++s)
            *(u32x4*)(sl + (4 * (a ^ (b & 1)) + pw) * 12288 + ((qb * 2 + c) * 2 + s) * 1024) = pack8n(acc[a][c][qb], s);
    if (pw == 0) STAMP(8);
  }
}

__global__ __launch_bounds__(256) void reduce_slabs(const u16* __restrict__ slab, u16* __restrict__ out) {
  const int gs = blockIdx.x & 7, bb = blockIdx.x >> 3;
  const int br = bb >> 4, b = bb & 15;
#pragma unroll
  for (int i = 0; i < 3; ++i) {
    const int ch = threadIdx.x + 256 * i;
    const int lane = ch & 63, f = ch >> 6;
    const int qb = f >> 2, c = (f >> 1) & 1, s = f & 1;
    float a[8] = {0.f, 0.f, 0.f, 0.f, 0.f, 0.f, 0.f, 0.f};
    u32x4 v[8];
#pragma unroll
    for (int g = 0; g < 8; ++g) v[g] = *(const u32x4*)((const char*)slab + (size_t)(bb * 8 + g) * 98304 + gs * 12288 + ch * 16);
#pragma unroll
    for (int g = 0; g < 8; ++g)
#pragma unroll
      for (int j = 0; j < 4; ++j) { a[2 * j] += __uint_as_float(v[g][j] << 16); a[2 * j + 1] += __uint_as_float(v[g][j] & 0xffff0000u); }
    u32x4 r = {cvtpk(a[0], a[1]), cvtpk(a[2], a[3]), cvtpk(a[4], a[5]), cvtpk(a[6], a[7])};
    const int q = 32 * qb + (lane & 31), n = 64 * gs + 32 * c + 16 * s + 8 * (lane >> 5);
    *(u32x4*)(out + ((size_t)(b * 96 + q)) * 1024 + br * 512 + n) = r;
  }
}

extern "C" void kernel_launch(void* const* d_in, const int* in_sizes, int n_in, void* d_out, int out_size, void* d_ws, size_t ws_size, hipStream_t stream) {
  (void)in_sizes; (void)n_in; (void)out_size; (void)ws_size;
  (void)hipFuncSetAttribute((const void*)fused_attn, hipFuncAttributeMaxDynamicSharedMemorySize, LDS_BYTES);
  u16* slab = (u16*)d_ws;
  hipLaunchKernelGGL(fused_attn, dim3(256), dim3(512), LDS_BYTES, stream,
                     (const u16*)d_in[0], (const u16*)d_in[1], (const u16*)d_in[2], (const u16*)d_in[3], (const u16*)d_in[4],
                     (const u16*)d_in[5], (const u16*)d_in[6], (const u16*)d_in[7], (const u16*)d_in[8], (const u16*)d_in[9], slab
  );
  hipLaunchKernelGGL(reduce_slabs, dim3(256), dim3(256), 0, stream, (const u16*)slab, (u16*)d_out);
}
```

```cpp
#include <hip/hip_runtime.h>
#include <stdint.h>

typedef unsigned short u16;
typedef short bf16x8 __attribute__((ext_vector_type(8)));
typedef short s16x4 __attribute__((ext_vector_type(4)));
typedef float f32x16 __attribute__((ext_vector_type(16)));
typedef float f32x2 __attribute__((ext_vector_type(2)));
typedef __bf16 bf16x2 __attribute__((ext_vector_type(2)));
typedef unsigned u32x4 __attribute__((ext_vector_type(4)));
#define LAS __attribute__((address_space(3)))
#define DI __device__ __forceinline__
typedef LAS const char* lds_cptr;
typedef LAS char* lds_ptr;
typedef __attribute__((address_space(1))) unsigned long long gu64;

constexpr int QO_OFF = 0;
constexpr int WO_OFF = 24576, WO_SLOT = 8192, WO_END = 65536;
constexpr int Q1_OFF = 40960;
constexpr int KV_OFF = 65536, KV_SLOT = 16384, KV_END = 163840;
constexpr int KV_FIRST = KV_OFF + 4 * KV_SLOT;
constexpr int WS_A = 0, WS_C = 49152, WS_B = 81920;
constexpr int XS_0 = 32768, XS_1 = 114688;
constexpr int R3B_OFF = 114688;
constexpr int LDS_BYTES = 163840;
constexpr size_t SLAB_BYTES = 98304;
constexpr size_t FLAG_OFF = 32u << 20;
constexpr float C_SCALE = 0.08838834764831845f * 1.4426950408889634f;
constexpr float THR = 6.0f;

#define WO_DFLT
#define SBAR() __builtin_amdgcn_sched_barrier(0)
#define PIN(x) asm volatile("" : "+v"(x))
#define MFMA(a, b, c) __builtin_amdgcn_mfma_f32_32x32x16_bf16((a), (b), (c), 0, 0, 0)

DI unsigned cvtpk(float lo, float hi) { f32x2 v = {lo, hi}; bf16x2 b = __builtin_convertvector(v, bf16x2); return __builtin_bit_cast(unsigned, b); }
DI bf16x8 ld128(lds_cptr p) { return *(const LAS bf16x8*)p; }
DI void st128(lds_ptr p, u32x4 v) { *(LAS u32x4*)p = v; }
DI s16x4 ldtr(lds_cptr p) { typedef short v4i16_t __attribute__((ext_vector_type(4))); return __builtin_bit_cast(s16x4, __builtin_amdgcn_ds_read_tr16_b64_v4i16((LAS v4i16_t*)p)); }
DI bf16x8 cat(s16x4 lo, s16x4 hi) { return __builtin_shufflevector(lo, hi, 0, 1, 2, 3, 4, 5, 6, 7); }
DI bf16x8 ldtr2(lds_cptr p, int second) { return cat(ldtr(p), ldtr(p + second)); }
DI u32x4 pack8s(const f32x16& x, int s, float sc) {
  u32x4 w = {cvtpk(x[8 * s] * sc, x[8 * s + 1] * sc), cvtpk(x[8 * s + 2] * sc, x[8 * s + 3] * sc), cvtpk(x[8 * s + 4] * sc, x[8 * s + 5] * sc), cvtpk(x[8 * s + 6] * sc, x[8 * s + 7] * sc)};
  return w;
}
DI u32x4 pack8n(const f32x16& x, int s) {
  u32x4 w = {cvtpk(x[8 * s], x[8 * s + 1]), cvtpk(x[8 * s + 2], x[8 * s + 3]), cvtpk(x[8 * s + 4], x[8 * s + 5]), cvtpk(x[8 * s + 6], x[8 * s + 7])};
  return w;
}
DI bf16x8 asfrag(u32x4 v) { return __builtin_bit_cast(bf16x8, v); }
DI void dma16(__amdgpu_buffer_rsrc_t srd, unsigned voff, unsigned soff, unsigned ldsaddr) {
  asm volatile("s_mov_b32 m0, %0\n\ts_nop 3\n\tbuffer_load_dwordx4 %1, %2, %3 offen lds" ::"s"(ldsaddr), "v"(voff), "s"(srd), "s"(soff) : "memory");
}
DI void dma16nt(__amdgpu_buffer_rsrc_t srd, unsigned voff, unsigned soff, unsigned ldsaddr) {
  asm volatile("s_mov_b32 m0, %0\n\ts_nop 3\n\tbuffer_load_dwordx4 %1, %2, %3 offen nt lds" ::"s"(ldsaddr), "v"(voff), "s"(srd), "s"(soff) : "memory");
}
DI int fresh_lane() { int l; asm volatile("v_mbcnt_lo_u32_b32 %0, -1, 0\n\tv_mbcnt_hi_u32_b32 %0, -1, %0" : "=v"(l)); return l; }
#define LANE_CONSTS() const int lane = fresh_lane(); const int l32 = lane & 31, h = lane >> 5; const int blk = (lane >> 4) & 1, q4 = (lane & 15) >> 2, p = lane & 3; \
  const unsigned r8 = (lane >> 2) & 7, slot4 = lane & 3; (void)l32; (void)h; (void)blk; (void)q4; (void)p; (void)r8; (void)slot4
#define WAITVM(n) asm volatile("s_waitcnt vmcnt(" #n ")" ::: "memory")

#define STAMP(i)
#define BAR() do { __builtin_amdgcn_sched_barrier(0); __builtin_amdgcn_s_waitcnt(0xC07F); asm volatile("s_barrier" ::: "memory"); } while (0)

DI float rowmax16(const f32x16& c) {
  float a = fmaxf(fmaxf(c[0], c[1]), c[2]), b = fmaxf(fmaxf(c[3], c[4]), c[5]);
  a = fmaxf(fmaxf(a, c[6]), c[7]); b = fmaxf(fmaxf(b, c[8]), c[9]);
  a = fmaxf(fmaxf(a, c[10]), c[11]); b = fmaxf(fmaxf(b, c[12]), c[13]);
  a = fmaxf(fmaxf(a, c[14]), c[15]);
  const float m = fmaxf(a, b);
  auto rr = __builtin_amdgcn_permlane32_swap(__float_as_uint(m), __float_as_uint(m), false, false);
  return fmaxf(__uint_as_float(rr[0]), __uint_as_float(rr[1]));
}

__global__ __launch_bounds__(512, 2) void fused_attn(const u16* __restrict__ xA, const u16* __restrict__ kA, const u16* __restrict__ vA, const u16* __restrict__ wqA,
                                                   const u16* __restrict__ woA, const u16* __restrict__ xB, const u16* __restrict__ kB, const u16* __restrict__ vB,
                                                   const u16* __restrict__ wqB, const u16* __restrict__ woB, char* __restrict__ ws, u16* __restrict__ out
) {
  extern __shared__ __attribute__((aligned(16))) char smem[];
  const lds_ptr lds = (lds_ptr)smem;
  const unsigned lds0 = (unsigned)(uintptr_t)smem;
  const int w = __builtin_amdgcn_readfirstlane(threadIdx.x >> 6);
  const int bid = blockIdx.x, xcd = bid & 7, idx = bid >> 3;
  const int br = xcd >> 2, g = 2 * (xcd & 3) + (idx & 1), b = idx >> 1;
  const unsigned krot = (idx + 2 * xcd) & 15;
  gu64* const flagp = (gu64*)(ws + FLAG_OFF + (size_t)(br * 16 + b) * 256);
  unsigned gen_lo = 0, gen_hi = 0;

  auto qphase = [&](f32x16 (&acc)[4][3], int head) __attribute__((always_inline)) {
    LANE_CONSTS();
    const unsigned wqb = 8192u * head + 2048u * h + 64u * q4 + 32u * blk + 16u * (p & 1) + 8u * (p >> 1);
    const unsigned xb0 = 1024u * (l32 >> 3) + 64u * (l32 & 7) + 16u * (h ^ ((l32 >> 2) & 3));
#pragma unroll
    for (int d = 0; d < 4; ++d)
#pragma unroll
      for (int q = 0; q < 3; ++q) acc[d][q] = f32x16{};
    bf16x8 wa[2], wb[2], x0[3], x1[3];
    if (w == 0) STAMP(13);
    BAR();
    if (w == 0) STAMP(12);
#define QMM(W, X, D0) do { _Pragma("unroll") for (int d_ = 0; d_ < 2; ++d_) _Pragma("unroll") for (int q_ = 0; q_ < 3; ++q_) acc[D0 + d_][q_] = MFMA(W[d_], X[q_], acc[D0 + d_][q_]); SBAR(); } while (0)
#define QRW(W, KS, D0) do { W[0] = ldtr2(wp + 4096 * KS + 512 * (D0), 256); W[1] = ldtr2(wp + 4096 * KS + 512 * (D0 + 1), 256); } while (0)
#define QSTEP(j, FIRST) do { \
      const lds_cptr wp = lds + st + wqb; \
      const int xs = (((j) & 2) ? XS_1 : XS_0) + 512 * ((j) & 1); \
      const lds_cptr xp0 = lds + xs + xb0, xp1 = lds + xs + (xb0 ^ 32u); \
      x0[0] = ld128(xp0); x0[1] = ld128(xp0 + 4096); x0[2] = ld128(xp0 + 8192); \
      QRW(wa, 0, 0); SBAR(); \
      if (!(FIRST)) QMM(wb, x1, 2); \
      QRW(wb, 0, 2); SBAR(); \
      QMM(wa, x0, 0); \
      x1[0] = ld128(xp1); x1[1] = ld128(xp1 + 4096); x1[2] = ld128(xp1 + 8192); \
      QRW(wa, 1, 0); SBAR(); \
      QMM(wb, x0, 2); \
      QRW(wb, 1, 2); SBAR(); \
      QMM(wa, x1, 0); \
      st = (st == WS_A) ? WS_C : (st == WS_C) ? WS_B : WS_A; \
      BAR(); } while (0)
    int st = WS_A;
    QSTEP(0, true);
#pragma unroll 1
    for (int j = 1; j < 16; ++j) {
      QSTEP(j, false);
    }
#undef QSTEP
    QMM(wb, x1, 2);
#undef QMM
#undef QRW
  };

  if (w < 3) {
    {
      { const int lane = fresh_lane(); (void)lane;
        if (w == 0) { STAMP(0); const unsigned long long g0 = __hip_atomic_load(flagp, __ATOMIC_RELAXED, __HIP_MEMORY_SCOPE_AGENT);
          gen_lo = __builtin_amdgcn_readfirstlane((unsigned)g0); gen_hi = __builtin_amdgcn_readfirstlane((unsigned)(g0 >> 32)); } }
      f32x16 acc[4][3];
      qphase(acc, (w < 2) ? w : 3);
      const int lane = fresh_lane();
      const int o0 = (w == 0) ? QO_OFF : (w == 1) ? Q1_OFF : WO_OFF, o2 = (w == 0) ? QO_OFF + 16384 : (w == 1) ? Q1_OFF + 16384 : R3B_OFF;
#pragma unroll
      for (int q = 0; q < 3; ++q) {
        const lds_ptr dst = lds + ((q < 2) ? o0 + q * 8192 : o2) + lane * 16;
#pragma unroll
        for (int d = 0; d < 4; ++d) { st128(dst + (2 * d) * 1024, pack8s(acc[d][q], 0, C_SCALE)); st128(dst + (2 * d + 1) * 1024, pack8s(acc[d][q], 1, C_SCALE)); }
      }
      BAR();
    }
    {
      const int qb = w;
      LANE_CONSTS();
      if (w == 0) STAMP(1);
      __builtin_amdgcn_s_setprio(3);
      const unsigned kb0 = 2048u * (l32 >> 3) + 64u * (l32 & 7) + 16u * (h ^ ((l32 >> 2) & 3));
      const unsigned vb0 = 8192u + 256u * h + 64u * q4 + 32u * blk + 8u * p;
      const lds_ptr qo = lds + QO_OFF + qb * 8192 + lane * 16;
      f32x16 o[4], pA, pB, negm;
      bf16x8 qf[8], kf[8], vf[8];
      u32x4 pw0, pw1;
      float lsum = 0.f;
      int kvp = KV_FIRST, kvc = KV_FIRST;
#pragma unroll
      for (int dt = 0; dt < 4; ++dt) o[dt] = f32x16{};
      auto nextslot = [](int s) { return (s == KV_END - KV_SLOT) ? KV_OFF : s + KV_SLOT; };
#define EX(v) __builtin_amdgcn_exp2f(v)
#define SETM(x) do { _Pragma("unroll") for (int r_ = 0; r_ < 16; ++r_) negm[r_] = (x); PIN(negm); } while (0)
#define KRD(i) kf[i] = ld128(((i & 1) ? kp1 : kp0) + 512 * (i >> 1))
#define VRD(i) vf[i] = ldtr2(vp + 4096 * ((i) >> 2) + 512 * ((i) & 3), 2048)
      {
#pragma unroll
        for (int s = 0; s < 8; ++s) qf[s] = ld128(qo + s * 1024);
        { const lds_cptr kp0 = lds + kvc + kb0, kp1 = lds + kvc + (kb0 ^ 32u);
#pragma unroll
          for (int s = 0; s < 8; ++s) KRD(s); }
        pA = MFMA(kf[0], qf[0], f32x16{});
#pragma unroll
        for (int s = 1; s < 8; ++s) pA = MFMA(kf[s], qf[s], pA);
        SBAR();
        kvc = nextslot(kvc);
        { const lds_cptr kp0 = lds + kvc + kb0, kp1 = lds + kvc + (kb0 ^ 32u);
#pragma unroll
          for (int s = 0; s < 8; ++s) KRD(s); }
        const float m0 = rowmax16(pA);
        SETM(-m0);
#pragma unroll
        for (int r = 0; r < 16; ++r) pA[r] = EX(pA[r] - m0);
        BAR();
      }
#define GAPA(i, C, P) do { VRD(i); if (MODE != 2) { if (i == 0) C = MFMA(kf[0], qf[0], MODE == 0 ? negm : f32x16{}); else C = MFMA(kf[i], qf[i], C); } \
        if (i < 7) { sacc += P[2 * i + 2]; sacc += P[2 * i + 3]; } \
        if (i < 4) pw0[i] = cvtpk(P[2 * i], P[2 * i + 1]); else pw1[i - 4] = cvtpk(P[2 * i], P[2 * i + 1]); \
        PIN(sacc); if (i < 4) PIN(pw0); else PIN(pw1); SBAR(); } while (0)
#define PVM(i) o[(i) & 3] = MFMA(vf[i], asfrag(((i) < 4) ? pw0 : pw1), o[(i) & 3])
#define STEP(C, P, MODE_) do { constexpr int MODE = MODE_; \
        const lds_cptr vp = lds + kvp + vb0; \
        kvp = kvc; kvc = nextslot(kvc); \
        const lds_cptr kp0 = lds + kvc + kb0, kp1 = lds + kvc + (kb0 ^ 32u); \
        float sacc = P[0] + P[1]; \
        GAPA(0, C, P); GAPA(1, C, P); GAPA(2, C, P); GAPA(3, C, P); GAPA(4, C, P); GAPA(5, C, P); GAPA(6, C, P); GAPA(7, C, P); \
        lsum += sacc; \
        PVM(0); PVM(1); SBAR(); \
        bool resc = false; float alpha = 1.f, lold = lsum; \
        if (MODE == 0) { \
          const float rm = rowmax16(C);                          \
          if (__builtin_expect(__any(rm > THR), 0)) { const float dl = fmaxf(rm, 0.f); alpha = __builtin_amdgcn_exp2f(-dl); lsum *= alpha; resc = true; \
            const float nm_ = negm[0] - dl; SETM(nm_); _Pragma("unroll") for (int r = 0; r < 16; ++r) C[r] -= dl; } \
        } else if (MODE == 1) { const float m0 = rowmax16(C); SETM(-m0); lsum = 0.f; _Pragma("unroll") for (int r = 0; r < 16; ++r) C[r] -= m0; } \
        SBAR(); \
        if (MODE != 2) { \
          KRD(0); KRD(1); PVM(2); C[0] = EX(C[0]); C[1] = EX(C[1]); C[2] = EX(C[2]); PIN(C); SBAR(); \
          KRD(2); PVM(3); C[3] = EX(C[3]); C[4] = EX(C[4]); C[5] = EX(C[5]); PIN(C); SBAR(); \
          KRD(3); KRD(4); PVM(4); C[6] = EX(C[6]); C[7] = EX(C[7]); C[8] = EX(C[8]); PIN(C); SBAR(); \
          KRD(5); PVM(5); C[9] = EX(C[9]); C[10] = EX(C[10]); C[11] = EX(C[11]); PIN(C); SBAR(); \
          KRD(6); PVM(6); C[12] = EX(C[12]); C[13] = EX(C[13]); PIN(C); SBAR(); \
          KRD(7); PVM(7); C[14] = EX(C[14]); C[15] = EX(C[15]); PIN(C); SBAR(); \
        } else { PVM(2); PVM(3); PVM(4); PVM(5); PVM(6); PVM(7); } \
        if (MODE == 0) { if (resc) { _Pragma("unroll") for (int dt = 0; dt < 4; ++dt) _Pragma("unroll") for (int r = 0; r < 16; ++r) o[dt][r] *= alpha; } } \
        else {   \
          auto rr = __builtin_amdgcn_permlane32_swap(__float_as_uint(lold), __float_as_uint(lold), false, false); \
          const float inv = 1.0f / (__uint_as_float(rr[0]) + __uint_as_float(rr[1])); \
          _Pragma("unroll") for (int dt = 0; dt < 4; ++dt) { st128(qo + (2 * dt) * 1024, pack8s(o[dt], 0, inv)); st128(qo + (2 * dt + 1) * 1024, pack8s(o[dt], 1, inv)); o[dt] = f32x16{}; } \
        } \
        BAR(); } while (0)
      for (int t = 0; t < 7; ++t) { STEP(pB, pA, 0); STEP(pA, pB, 0); }
      STEP(pB, pA, 0);
      for (int u = 1; u < 4; ++u) {
        const lds_cptr qsrc = (u == 1) ? (lds + Q1_OFF + qb * 8192 + lane * 16) : (lds_cptr)qo;
#pragma unroll
        for (int s = 0; s < 8; ++s) qf[s] = ld128(qsrc + s * 1024);
        asm volatile("s_waitcnt lgkmcnt(0)" ::: "memory");
        STEP(pA, pB, 1);
        if (w == 0) STAMP(1 + u);
        for (int t = 0; t < 7; ++t) { STEP(pB, pA, 0); STEP(pA, pB, 0); }
        STEP(pB, pA, 0);
      }
      STEP(pA, pB, 2);
      if (w == 0) STAMP(5);
#undef STEP
#undef PVM
#undef GAPA
#undef VRD
#undef KRD
#undef EX
#undef SETM
      BAR();
    }
  } else if (w == 3) {
    bf16x8 q2[2][3][8];
    {
      f32x16 acc[4][3];
      qphase(acc, 2);
#pragma unroll
      for (int q = 0; q < 3; ++q)
#pragma unroll
        for (int d = 0; d < 4; ++d) { q2[0][q][2 * d] = asfrag(pack8s(acc[d][q], 0, C_SCALE)); q2[0][q][2 * d + 1] = asfrag(pack8s(acc[d][q], 1, C_SCALE)); }
      BAR();
    }
    {
      LANE_CONSTS();
      const u16* kg = br ? kB : kA; const u16* vg = br ? vB : vA; const u16* wog = br ? woB : woA;
      const size_t kvbase = ((size_t)b * 32 + 4 * g) * (512 * 128);
      const __amdgpu_buffer_rsrc_t srdK = __builtin_amdgcn_make_buffer_rsrc((void*)(kg + kvbase), 0, 4 * 512 * 128 * 2, 0x00020000);
      const __amdgpu_buffer_rsrc_t srdV = __builtin_amdgcn_make_buffer_rsrc((void*)(vg + kvbase), 0, 4 * 512 * 128 * 2, 0x00020000);
      const __amdgpu_buffer_rsrc_t srdWo = __builtin_amdgcn_make_buffer_rsrc((void*)(wog + (size_t)g * 512 * 512), 0, 512 * 512 * 2, 0x00020000);
      const unsigned voffV = r8 * 256u + 64u * h + 16u * slot4;
      const unsigned voffK0 = r8 * 256u + 64u * h + 16u * (slot4 ^ (r8 >> 2)), voffK1 = voffK0 ^ 32u;
      const unsigned voffWo = r8 * 1024u + 64u * h + 16u * slot4;
      auto issue_kv = [&](int T, int slot) {
        const unsigned so = (((unsigned)T & ~15u) | (((unsigned)T + krot) & 15u)) * 8192u, dst = lds0 + slot;
#pragma unroll
        for (int i = 0; i < 8; ++i) dma16nt(srdK, ((i >> 1) & 1) ? voffK1 : voffK0, so + 2048u * (i >> 1) + 128u * (i & 1), dst + 1024u * i);
#pragma unroll
        for (int i = 0; i < 8; ++i) dma16nt(srdV, voffV, so + 2048u * (i >> 1) + 128u * (i & 1), dst + 8192u + 1024u * i);
      };
      auto issue_wo16 = [&](int r, int slot) {
        const unsigned rr = (r + (b >> 1)) & 7;
        const unsigned so = (384u + 16u * rr) * 1024u, dst = lds0 + slot;
#pragma unroll
        for (int i = 0; i < 16; ++i) dma16(srdWo, voffWo, so + 512u * (i >> 3) + 8192u * ((i >> 2) & 1) + 128u * (i & 3), dst + 1024u * i);
      };
      {
        const lds_cptr h0 = lds + WO_OFF + lane * 16, h2 = lds + R3B_OFF + lane * 16;
#pragma unroll
        for (int s = 0; s < 8; ++s) { q2[1][0][s] = ld128(h0 + s * 1024); q2[1][1][s] = ld128(h0 + 8192 + s * 1024); q2[1][2][s] = ld128(h2 + s * 1024); }
      }
      BAR();
      BAR();
      int islot = KV_FIRST;
      const lds_ptr qo = lds + QO_OFF + lane * 16;
      for (int T = 2; T < 60; ++T) {
        issue_kv(T + 4, islot);
        islot = (islot == KV_END - KV_SLOT) ? KV_OFF : islot + KV_SLOT;
        if (T == 31) {
#pragma unroll
          for (int q = 0; q < 3; ++q)
#pragma unroll
            for (int s = 0; s < 8; ++s) st128(qo + q * 8192 + s * 1024, __builtin_bit_cast(u32x4, q2[0][q][s]));
        }
        if (T == 47) {
#pragma unroll
          for (int q = 0; q < 3; ++q)
#pragma unroll
            for (int s = 0; s < 8; ++s) st128(qo + q * 8192 + s * 1024, __builtin_bit_cast(u32x4, q2[1][q][s]));
        }
        WAITVM(32);
        BAR();
      }
      for (int r = 0; r < 5; ++r) {
        issue_wo16(r, islot);
        islot = (islot == KV_END - KV_SLOT) ? KV_OFF : islot + KV_SLOT;
        if (r < 4) WAITVM(32); else WAITVM(0);
        BAR();
      }
      issue_wo16(6, islot);
      WAITVM(0);
      BAR();
    }
  } else {
    const int pw = w - 4;
    const u16* wog = br ? woB : woA;
    {
      LANE_CONSTS();
      const u16* xg = br ? xB : xA; const u16* wqg = br ? wqB : wqA; const u16* kg = br ? kB : kA; const u16* vg = br ? vB : vA;
      const size_t kvbase = ((size_t)b * 32 + 4 * g) * (512 * 128);
      const __amdgpu_buffer_rsrc_t srdWq = __builtin_amdgcn_make_buffer_rsrc((void*)(wqg + g * 512), 0, 512 * 4096 * 2 - g * 1024, 0x00020000);
      const __amdgpu_buffer_rsrc_t srdX = __builtin_amdgcn_make_buffer_rsrc((void*)(xg + (size_t)b * 96 * 512), 0, 96 * 512 * 2, 0x00020000);
      const __amdgpu_buffer_rsrc_t srdK = __builtin_amdgcn_make_buffer_rsrc((void*)(kg + kvbase), 0, 4 * 512 * 128 * 2, 0x00020000);
      const __amdgpu_buffer_rsrc_t srdV = __builtin_amdgcn_make_buffer_rsrc((void*)(vg + kvbase), 0, 4 * 512 * 128 * 2, 0x00020000);
      const unsigned voffWq = r8 * 8192u + 64u * h + 16u * slot4;
      const unsigned voffX0 = r8 * 1024u + 64u * h + 16u * (slot4 ^ (r8 >> 2)), voffX1 = voffX0 ^ 32u;
      const unsigned voffK = (8u * pw + r8) * 256u + 64u * h + 16u * (slot4 ^ (r8 >> 2) ^ (2u * (pw & 1)));
      const unsigned voffV = (8u * pw + r8) * 256u + 64u * h + 16u * slot4;
      const unsigned rot = 2u * (b & 7);
      auto issue_w = [&](int j, int st) {
        const unsigned jj = (j + rot) & 15;
        const unsigned so = jj * (32u * 8192u) + pw * 256u, dst = lds0 + st + 8192u * pw;
#pragma unroll
        for (int i = 0; i < 8; ++i) dma16nt(srdWq, voffWq, so + 65536u * (i >> 1) + 128u * (i & 1), dst + 1024u * i);
      };
      auto issue_x = [&](int j) {
        const unsigned jj = (j + rot) & 15;
        const unsigned dst = lds0 + ((j & 2) ? XS_1 : XS_0) + 3072u * pw;
#pragma unroll
        for (int i = 0; i < 3; ++i) dma16nt(srdX, ((3 * pw + i) & 1) ? voffX1 : voffX0, jj * 64u + (3u * pw + i) * 8192u, dst + 1024u * i);
      };
      auto issue_t = [&](int T) {
        const unsigned so = (((unsigned)T + krot) & 15u) * 8192u, dst = lds0 + KV_OFF + ((T + 4) % 6) * KV_SLOT + 2048u * pw;
        dma16nt(srdK, voffK, so, dst); dma16nt(srdK, voffK, so + 128u, dst + 1024u);
        dma16nt(srdV, voffV, so, dst + 8192u); dma16nt(srdV, voffV, so + 128u, dst + 8192u + 1024u);
      };
      auto nextst = [](int st) { return (st == WS_A) ? WS_C : (st == WS_C) ? WS_B : WS_A; };
      issue_x(0);
      issue_w(0, WS_A);
      issue_w(1, WS_C);
      issue_t(0);
      issue_t(1);
      WAITVM(16);
      BAR();
      int st2 = WS_B;
      issue_x(2); issue_w(2, st2); st2 = nextst(st2); WAITVM(19); BAR();
      for (int j = 1; j < 13; j += 2) {
        issue_w(j + 2, st2); st2 = nextst(st2); WAITVM(8); BAR();
        issue_x(j + 3); issue_w(j + 3, st2); st2 = nextst(st2); WAITVM(11); BAR();
      }
      issue_w(15, st2); WAITVM(8); BAR();
      issue_t(2); WAITVM(4); BAR();
      issue_t(3); issue_t(4); WAITVM(12); BAR();
      BAR();
      WAITVM(8); BAR();
      issue_t(5); WAITVM(8); BAR();
      WAITVM(4); BAR();
      WAITVM(0); BAR();
      for (int T = 4; T < 15; ++T) BAR();
    }
    LANE_CONSTS();
    const __amdgpu_buffer_rsrc_t srdWo = __builtin_amdgcn_make_buffer_rsrc((void*)(wog + (size_t)g * 512 * 512), 0, 512 * 512 * 2, 0x00020000);
    const unsigned voffWo = (8u * (pw >> 1) + r8) * 1024u + 64u * (4u * (pw & 1) + h) + 16u * slot4;
    auto issue_wo = [&](int u, int m, int slot) {
      const unsigned rr = ((m >> 1) + (b >> 1)) & 7;
      const unsigned so = (128u * u + 16u * rr) * 1024u + ((m ^ b) & 1) * 512u;
      const unsigned dst = lds0 + WO_OFF + slot + 2048u * pw;
      dma16(srdWo, voffWo, so, dst);
      dma16(srdWo, voffWo, so + 128u, dst + 1024u);
    };
    const unsigned ob0 = 256u * h + 64u * q4 + 32u * blk + 16u * (p & 1) + 8u * (p >> 1) + 1024u * pw;
    const lds_cptr qo = lds + QO_OFF + lane * 16;
    f32x16 acc[2][2][3];
#pragma unroll
    for (int a = 0; a < 2; ++a)
#pragma unroll
      for (int c = 0; c < 2; ++c)
#pragma unroll
        for (int qb = 0; qb < 3; ++qb) acc[a][c][qb] = f32x16{};
    bf16x8 of[3], ofn[3];
    auto issue_M = [&](int M, int slot) { if (M < 48) issue_wo(M >> 4, M & 15, slot); else issue_wo(3, 10 + (M & 1) + 2 * (M & 2), slot); };
    issue_wo(0, 0, 0 * WO_SLOT);  BAR();
    issue_wo(0, 1, 1 * WO_SLOT);  WAITVM(2); BAR();
    issue_wo(0, 2, 2 * WO_SLOT); issue_wo(0, 3, 3 * WO_SLOT);
    int cur = 0, nxt = 4 * WO_SLOT;
    auto rot = [&]() { cur = (cur == 4 * WO_SLOT) ? 0 : cur + WO_SLOT; nxt = (nxt == 4 * WO_SLOT) ? 0 : nxt + WO_SLOT; };
    const int rb = b >> 1;
    for (int M = 0; M < 48; M += 2) {
      const int m = M & 15;
      {
        issue_M(M + 4, nxt);
        if (m == 0) {
#pragma unroll
          for (int qb = 0; qb < 3; ++qb) of[qb] = ld128(qo + qb * 8192 + (rb & 7) * 1024);
        }
        const lds_cptr wp = lds + WO_OFF + cur + ob0;
#pragma unroll
        for (int c = 0; c < 2; ++c) {
          const bf16x8 wf = ldtr2(wp + 512 * c, 4096);
#pragma unroll
          for (int qb = 0; qb < 3; ++qb) acc[0][c][qb] = MFMA(wf, of[qb], acc[0][c][qb]);
        }
        WAITVM(6);
        BAR();
        rot();
      }
      {
        issue_M(M + 5, nxt);
        if (m != 14) {
#pragma unroll
          for (int qb = 0; qb < 3; ++qb) ofn[qb] = ld128(qo + qb * 8192 + (((m >> 1) + 1 + rb) & 7) * 1024);
        }
        const lds_cptr wp = lds + WO_OFF + cur + ob0;
#pragma unroll
        for (int c = 0; c < 2; ++c) {
          const bf16x8 wf = ldtr2(wp + 512 * c, 4096);
#pragma unroll
          for (int qb = 0; qb < 3; ++qb) acc[1][c][qb] = MFMA(wf, of[qb], acc[1][c][qb]);
        }
#pragma unroll
        for (int qb = 0; qb < 3; ++qb) of[qb] = ofn[qb];
        if (M < 46) WAITVM(6); else WAITVM(0);
        BAR();
        rot();
      }
    }
    if (pw == 0) STAMP(7);
    {
      const unsigned hsel = (b & 1) * 8192u;
      auto range = [&](int r, lds_cptr wp0, lds_cptr wp1) __attribute__((always_inline)) {
#pragma unroll
        for (int qb = 0; qb < 3; ++qb) of[qb] = ld128(qo + qb * 8192 + ((r + rb) & 7) * 1024);
#pragma unroll
        for (int c = 0; c < 2; ++c) {
          const bf16x8 wf0 = ldtr2(wp0 + 512 * c, 4096), wf1 = ldtr2(wp1 + 512 * c, 4096);
#pragma unroll
          for (int qb = 0; qb < 3; ++qb) { acc[0][c][qb] = MFMA(wf0, of[qb], acc[0][c][qb]); acc[1][c][qb] = MFMA(wf1, of[qb], acc[1][c][qb]); }
        }
      };
      int sl = KV_OFF + ((58 + 4) % 6) * KV_SLOT;
      for (int r = 0; r < 5; ++r) {
        range(r, lds + sl + ob0 + hsel, lds + sl + ob0 + (hsel ^ 8192u));
        sl = (sl == KV_END - KV_SLOT) ? KV_OFF : sl + KV_SLOT;
      }
      auto nx = [](int s) { return (s == 4 * WO_SLOT) ? 0 : s + WO_SLOT; };
      const int c1 = nx(cur), c2 = nx(c1), c3 = nx(c2);
      range(5, lds + WO_OFF + cur + ob0, lds + WO_OFF + c1 + ob0);
      range(7, lds + WO_OFF + c2 + ob0, lds + WO_OFF + c3 + ob0);
      BAR();
      range(6, lds + sl + ob0 + hsel, lds + sl + ob0 + (hsel ^ 8192u));
    }
    const __amdgpu_buffer_rsrc_t srdS = __builtin_amdgcn_make_buffer_rsrc((void*)(ws + (size_t)((br * 16 + b) * 8 + g) * SLAB_BYTES), 0, (int)SLAB_BYTES, 0x00020000);
#pragma unroll
    for (int a = 0; a < 2; ++a)
#pragma unroll
      for (int qb = 0; qb < 3; ++qb)
#pragma unroll
        for (int c = 0; c < 2; ++c)
#pragma unroll
          for (int s = 0; s < 2; ++s)
            __builtin_amdgcn_raw_buffer_store_b128(pack8n(acc[a][c][qb], s), srdS, lane * 16, (4 * (a ^ (b & 1)) + pw) * 12288 + ((qb * 2 + c) * 2 + s) * 1024, 16);
    asm volatile("s_waitcnt vmcnt(0)" ::: "memory");
    if (pw == 0) STAMP(8);
  }
  asm volatile("s_waitcnt lgkmcnt(0)\n\ts_barrier" ::: "memory");
  LANE_CONSTS();
  if (w == 0) {
    const unsigned long long want = (((unsigned long long)gen_hi << 32) | gen_lo) + 1;
    if (lane == 0) __hip_atomic_store(flagp + 16 + g, want, __ATOMIC_RELAXED, __HIP_MEMORY_SCOPE_AGENT);
    for (unsigned spins = 0; spins < (1u << 22); ++spins) {
      const unsigned long long f = __hip_atomic_load(flagp + 16 + (lane & 7), __ATOMIC_RELAXED, __HIP_MEMORY_SCOPE_AGENT);
      if (__all(f == want)) break;
      __builtin_amdgcn_s_sleep(1);
    }
    if (g == 0 && lane == 0) __hip_atomic_store(flagp, want, __ATOMIC_RELAXED, __HIP_MEMORY_SCOPE_AGENT);
    STAMP(9);
  }
  asm volatile("s_waitcnt lgkmcnt(0)\n\ts_barrier" ::: "memory");
  {
    const __amdgpu_buffer_rsrc_t srdG = __builtin_amdgcn_make_buffer_rsrc((void*)(ws + (size_t)((br * 16 + b) * 8) * SLAB_BYTES), 0, (int)(8 * SLAB_BYTES), 0x00020000);
    const int nch = (w < 4) ? 2 : 1;
    u32x4 v[2][8];
#pragma unroll
    for (int i = 0; i < 2; ++i)
      if (i < nch) {
#pragma unroll
        for (int s = 0; s < 8; ++s) v[i][s] = __builtin_amdgcn_raw_buffer_load_b128(srdG, lane * 16, s * (int)SLAB_BYTES + g * 12288 + (w + 8 * i) * 1024, 16);
      }
#pragma unroll
    for (int i = 0; i < 2; ++i)
      if (i < nch) {
        float a[8] = {0.f, 0.f, 0.f, 0.f, 0.f, 0.f, 0.f, 0.f};
#pragma unroll
        for (int s = 0; s < 8; ++s)
#pragma unroll
          for (int j = 0; j < 4; ++j) { a[2 * j] += __uint_as_float(v[i][s][j] << 16); a[2 * j + 1] += __uint_as_float(v[i][s][j] & 0xffff0000u); }
        const u32x4 r = {cvtpk(a[0], a[1]), cvtpk(a[2], a[3]), cvtpk(a[4], a[5]), cvtpk(a[6], a[7])};
        const int f = w + 8 * i, qb = f >> 2, c = (f >> 1) & 1, s = f & 1;
        const int q = 32 * qb + l32, n = 64 * g + 32 * c + 16 * s + 8 * h;
        *(u32x4*)(out + ((size_t)(b * 96 + q)) * 1024 + br * 512 + n) = r;
      }
  }
}

extern "C" void kernel_launch(void* const* d_in, const int* in_sizes, int n_in, void* d_out, int out_size, void* d_ws, size_t ws_size, hipStream_t stream) {
  (void)in_sizes; (void)n_in; (void)out_size; (void)ws_size;
  (void)hipFuncSetAttribute((const void*)fused_attn, hipFuncAttributeMaxDynamicSharedMemorySize, LDS_BYTES);
  hipLaunchKernelGGL(fused_attn, dim3(256), dim3(512), LDS_BYTES, stream,
                     (const u16*)d_in[0], (const u16*)d_in[1], (const u16*)d_in[2], (const u16*)d_in[3], (const u16*)d_in[4],
                     (const u16*)d_in[5], (const u16*)d_in[6], (const u16*)d_in[7], (const u16*)d_in[8], (const u16*)d_in[9], (char*)d_ws, (u16*)d_out
  );
}
```

```cpp
#include <hip/hip_runtime.h>
#include <stdint.h>

typedef unsigned short u16;
typedef short bf16x8 __attribute__((ext_vector_type(8)));
typedef short s16x4 __attribute__((ext_vector_type(4)));
typedef float f32x16 __attribute__((ext_vector_type(16)));
typedef float f32x2 __attribute__((ext_vector_type(2)));
typedef __bf16 bf16x2 __attribute__((ext_vector_type(2)));
typedef unsigned u32x4 __attribute__((ext_vector_type(4)));
#define LAS __attribute__((address_space(3)))
#define DI __device__ __forceinline__
typedef LAS const char* lds_cptr;
typedef LAS char* lds_ptr;
typedef __attribute__((address_space(1))) unsigned long long gu64;

constexpr int QO_OFF = 0;
constexpr int WO_OFF = 24576, WO_SLOT = 8192, WO_END = 65536;
constexpr int Q1_OFF = 40960;
constexpr int KV_OFF = 65536, KV_SLOT = 16384, KV_END = 163840;
constexpr int KV_FIRST = KV_OFF + 4 * KV_SLOT;
constexpr int WS_A = 0, WS_C = 49152, WS_B = 81920;
constexpr int XS_0 = 32768, XS_1 = 114688;
constexpr int R3B_OFF = 114688;
constexpr int LDS_BYTES = 163840;
constexpr size_t SLAB_BYTES = 98304;
constexpr size_t FLAG_OFF = 32u << 20;
constexpr float C_SCALE = 0.08838834764831845f * 1.4426950408889634f;
constexpr float THR = 6.0f;

#define PMN 3
#define WO_DFLT
#define SBAR() __builtin_amdgcn_sched_barrier(0)
#define PIN(x) asm volatile("" : "+v"(x))
#define MFMA(a, b, c) __builtin_amdgcn_mfma_f32_32x32x16_bf16((a), (b), (c), 0, 0, 0)

DI unsigned cvtpk(float lo, float hi) { f32x2 v = {lo, hi}; bf16x2 b = __builtin_convertvector(v, bf16x2); return __builtin_bit_cast(unsigned, b); }
DI bf16x8 ld128(lds_cptr p) { return *(const LAS bf16x8*)p; }
DI void st128(lds_ptr p, u32x4 v) { *(LAS u32x4*)p = v; }
DI s16x4 ldtr(lds_cptr p) { typedef short v4i16_t __attribute__((ext_vector_type(4))); return __builtin_bit_cast(s16x4, __builtin_amdgcn_ds_read_tr16_b64_v4i16((LAS v4i16_t*)p)); }
DI bf16x8 cat(s16x4 lo, s16x4 hi) { return __builtin_shufflevector(lo, hi, 0, 1, 2, 3, 4, 5, 6, 7); }
DI bf16x8 ldtr2(lds_cptr p, int second) { return cat(ldtr(p), ldtr(p + second)); }
DI u32x4 pack8s(const f32x16& x, int s, float sc) {
  u32x4 w = {cvtpk(x[8 * s] * sc, x[8 * s + 1] * sc), cvtpk(x[8 * s + 2] * sc, x[8 * s + 3] * sc), cvtpk(x[8 * s + 4] * sc, x[8 * s + 5] * sc), cvtpk(x[8 * s + 6] * sc, x[8 * s + 7] * sc)};
  return w;
}
DI u32x4 pack8n(const f32x16& x, int s) {
  u32x4 w = {cvtpk(x[8 * s], x[8 * s + 1]), cvtpk(x[8 * s + 2], x[8 * s + 3]), cvtpk(x[8 * s + 4], x[8 * s + 5]), cvtpk(x[8 * s + 6], x[8 * s + 7])};
  return w;
}
DI bf16x8 asfrag(u32x4 v) { return __builtin_bit_cast(bf16x8, v); }
DI void dma16(__amdgpu_buffer_rsrc_t srd, unsigned voff, unsigned soff, unsigned ldsaddr) {
  asm volatile("s_mov_b32 m0, %0\n\ts_nop 3\n\tbuffer_load_dwordx4 %1, %2, %3 offen lds" ::"s"(ldsaddr), "v"(voff), "s"(srd), "s"(soff) : "memory");
}
DI void dma16nt(__amdgpu_buffer_rsrc_t srd, unsigned voff, unsigned soff, unsigned ldsaddr) {
  asm volatile("s_mov_b32 m0, %0\n\ts_nop 3\n\tbuffer_load_dwordx4 %1, %2, %3 offen nt lds" ::"s"(ldsaddr), "v"(voff), "s"(srd), "s"(soff) : "memory");
}
DI int fresh_lane() { int l; asm volatile("v_mbcnt_lo_u32_b32 %0, -1, 0\n\tv_mbcnt_hi_u32_b32 %0, -1, %0" : "=v"(l)); return l; }
#define LANE_CONSTS() const int lane = fresh_lane(); const int l32 = lane & 31, h = lane >> 5; const int blk = (lane >> 4) & 1, q4 = (lane & 15) >> 2, p = lane & 3; \
  const unsigned r8 = (lane >> 2) & 7, slot4 = lane & 3; (void)l32; (void)h; (void)blk; (void)q4; (void)p; (void)r8; (void)slot4
#define WAITVM(n) asm volatile("s_waitcnt vmcnt(" #n ")" ::: "memory")

#define STAMP(i)
#define BAR() do { __builtin_amdgcn_sched_barrier(0); __builtin_amdgcn_s_waitcnt(0xC07F); asm volatile("s_barrier" ::: "memory"); } while (0)

DI float rowmax16(const f32x16& c) {
  float a = fmaxf(fmaxf(c[0], c[1]), c[2]), b = fmaxf(fmaxf(c[3], c[4]), c[5]);
  a = fmaxf(fmaxf(a, c[6]), c[7]); b = fmaxf(fmaxf(b, c[8]), c[9]);
  a = fmaxf(fmaxf(a, c[10]), c[11]); b = fmaxf(fmaxf(b, c[12]), c[13]);
  a = fmaxf(fmaxf(a, c[14]), c[15]);
  const float m = fmaxf(a, b);
  auto rr = __builtin_amdgcn_permlane32_swap(__float_as_uint(m), __float_as_uint(m), false, false);
  return fmaxf(__uint_as_float(rr[0]), __uint_as_float(rr[1]));
}

__global__ __launch_bounds__(512, 2) void fused_attn(const u16* __restrict__ xA, const u16* __restrict__ kA, const u16* __restrict__ vA, const u16* __restrict__ wqA,
                                                   const u16* __restrict__ woA, const u16* __restrict__ xB, const u16* __restrict__ kB, const u16* __restrict__ vB,
                                                   const u16* __restrict__ wqB, const u16* __restrict__ woB, char* __restrict__ ws, u16* __restrict__ out
) {
  extern __shared__ __attribute__((aligned(16))) char smem[];
  const lds_ptr lds = (lds_ptr)smem;
  const unsigned lds0 = (unsigned)(uintptr_t)smem;
  const int w = __builtin_amdgcn_readfirstlane(threadIdx.x >> 6);
  const int bid = blockIdx.x, xcd = bid & 7, idx = bid >> 3;
  const int br = xcd >> 2, g = 2 * (xcd & 3) + (idx & 1), b = idx >> 1;
  const unsigned krot = (idx + 2 * xcd) & 15;
  gu64* const flagp = (gu64*)(ws + FLAG_OFF + (size_t)(br * 16 + b) * 256);
  unsigned gen_lo = 0, gen_hi = 0;

  auto qphase = [&](f32x16 (&acc)[4][3], int head) __attribute__((always_inline)) {
    LANE_CONSTS();
    const unsigned wqb = 8192u * head + 2048u * h + 64u * q4 + 32u * blk + 16u * (p & 1) + 8u * (p >> 1);
    const unsigned xb0 = 1024u * (l32 >> 3) + 64u * (l32 & 7) + 16u * (h ^ ((l32 >> 2) & 3));
#pragma unroll
    for (int d = 0; d < 4; ++d)
#pragma unroll
      for (int q = 0; q < 3; ++q) acc[d][q] = f32x16{};
    bf16x8 wa[2], wb[2], x0[3], x1[3];
    if (w == 0) STAMP(13);
    BAR();
    if (w == 0) STAMP(12);
#define QMM(W, X, D0) do { _Pragma("unroll") for (int d_ = 0; d_ < 2; ++d_) _Pragma("unroll") for (int q_ = 0; q_ < 3; ++q_) acc[D0 + d_][q_] = MFMA(W[d_], X[q_], acc[D0 + d_][q_]); SBAR(); } while (0)
#define QRW(W, KS, D0) do { W[0] = ldtr2(wp + 4096 * KS + 512 * (D0), 256); W[1] = ldtr2(wp + 4096 * KS + 512 * (D0 + 1), 256); } while (0)
#define QSTEP(j, FIRST) do { \
      const lds_cptr wp = lds + st + wqb; \
      const int xs = (((j) & 2) ? XS_1 : XS_0) + 512 * ((j) & 1); \
      const lds_cptr xp0 = lds + xs + xb0, xp1 = lds + xs + (xb0 ^ 32u); \
      x0[0] = ld128(xp0); x0[1] = ld128(xp0 + 4096); x0[2] = ld128(xp0 + 8192); \
      QRW(wa, 0, 0); SBAR(); \
      if (!(FIRST)) QMM(wb, x1, 2); \
      QRW(wb, 0, 2); SBAR(); \
      QMM(wa, x0, 0); \
      x1[0] = ld128(xp1); x1[1] = ld128(xp1 + 4096); x1[2] = ld128(xp1 + 8192); \
      QRW(wa, 1, 0); SBAR(); \
      QMM(wb, x0, 2); \
      QRW(wb, 1, 2); SBAR(); \
      QMM(wa, x1, 0); \
      st = (st == WS_A) ? WS_C : (st == WS_C) ? WS_B : WS_A; \
      BAR(); } while (0)
    int st = WS_A;
    QSTEP(0, true);
#pragma unroll 1
    for (int j = 1; j < 16; ++j) {
      QSTEP(j, false);
    }
#undef QSTEP
    QMM(wb, x1, 2);
#undef QMM
#undef QRW
  };

  if (w < 3) {
    {
      { const int lane = fresh_lane(); (void)lane;
      if (w == 0) { STAMP(0); const unsigned long long g0 = __hip_atomic_load(flagp, __ATOMIC_RELAXED, __HIP_MEMORY_SCOPE_AGENT);
          gen_lo = __builtin_amdgcn_readfirstlane((unsigned)g0); gen_hi = __builtin_amdgcn_readfirstlane((unsigned)(g0 >> 32)); } }
      f32x16 acc[4][3];
      qphase(acc, (w < 2) ? w : 3);
      const int lane = fresh_lane();
      const int o0 = (w == 0) ? QO_OFF : (w == 1) ? Q1_OFF : WO_OFF, o2 = (w == 0) ? QO_OFF + 16384 : (w == 1) ? Q1_OFF + 16384 : R3B_OFF;
#pragma unroll
      for (int q = 0; q < 3; ++q) {
        const lds_ptr dst = lds + ((q < 2) ? o0 + q * 8192 : o2) + lane * 16;
#pragma unroll
        for (int d = 0; d < 4; ++d) { st128(dst + (2 * d) * 1024, pack8s(acc[d][q], 0, C_SCALE)); st128(dst + (2 * d + 1) * 1024, pack8s(acc[d][q], 1, C_SCALE)); }
      }
      BAR();
    }
    {
      const int qb = w;
      LANE_CONSTS();
      if (w == 0) STAMP(1);
      __builtin_amdgcn_s_setprio(3);
      const unsigned kb0 = 2048u * (l32 >> 3) + 64u * (l32 & 7) + 16u * (h ^ ((l32 >> 2) & 3));
      const unsigned vb0 = 8192u + 256u * h + 64u * q4 + 32u * blk + 8u * p;
      const lds_ptr qo = lds + QO_OFF + qb * 8192 + lane * 16;
      f32x16 o[4], pA, pB, negm;
      bf16x8 qf[8], kf[8], vf[8];
      u32x4 pw0, pw1;
      float lsum = 0.f;
      int kvp = KV_FIRST, kvc = KV_FIRST;
#pragma unroll
      for (int dt = 0; dt < 4; ++dt) o[dt] = f32x16{};
      auto nextslot = [](int s) { return (s == KV_END - KV_SLOT) ? KV_OFF : s + KV_SLOT; };
#define EX(v) __builtin_amdgcn_exp2f(v)
#define SETM(x) do { _Pragma("unroll") for (int r_ = 0; r_ < 16; ++r_) negm[r_] = (x); PIN(negm); } while (0)
#define KRD(i) kf[i] = ld128(((i & 1) ? kp1 : kp0) + 512 * (i >> 1))
#define VRD(i) vf[i] = ldtr2(vp + 4096 * ((i) >> 2) + 512 * ((i) & 3), 2048)
      {
#pragma unroll
        for (int s = 0; s < 8; ++s) qf[s] = ld128(qo + s * 1024);
        { const lds_cptr kp0 = lds + kvc + kb0, kp1 = lds + kvc + (kb0 ^ 32u);
#pragma unroll
          for (int s = 0; s < 8; ++s) KRD(s); }
        pA = MFMA(kf[0], qf[0], f32x16{});
#pragma unroll
        for (int s = 1; s < 8; ++s) pA = MFMA(kf[s], qf[s], pA);
        SBAR();
        kvc = nextslot(kvc);
        { const lds_cptr kp0 = lds + kvc + kb0, kp1 = lds + kvc + (kb0 ^ 32u);
#pragma unroll
          for (int s = 0; s < 8; ++s) KRD(s); }
        const float m0 = rowmax16(pA);
        SETM(-m0);
#pragma unroll
        for (int r = 0; r < 16; ++r) pA[r] = EX(pA[r] - m0);
        BAR();
      }
#define GAPA(i, C, P) do { VRD(i); if (MODE != 2) { if (i == 0) C = MFMA(kf[0], qf[0], MODE == 0 ? negm : f32x16{}); else C = MFMA(kf[i], qf[i], C); } \
        if (i < 7) { sacc += P[2 * i + 2]; sacc += P[2 * i + 3]; } \
        if (i < 4) pw0[i] = cvtpk(P[2 * i], P[2 * i + 1]); else pw1[i - 4] = cvtpk(P[2 * i], P[2 * i + 1]); \
        PIN(sacc); if (i < 4) PIN(pw0); else PIN(pw1); SBAR(); } while (0)
#define PVM(i) o[(i) & 3] = MFMA(vf[i], asfrag(((i) < 4) ? pw0 : pw1), o[(i) & 3])
#define STEP(C, P, MODE_) do { constexpr int MODE = MODE_; \
        const lds_cptr vp = lds + kvp + vb0; \
        kvp = kvc; kvc = nextslot(kvc); \
        const lds_cptr kp0 = lds + kvc + kb0, kp1 = lds + kvc + (kb0 ^ 32u); \
        float sacc = P[0] + P[1]; \
        GAPA(0, C, P); GAPA(1, C, P); GAPA(2, C, P); GAPA(3, C, P); GAPA(4, C, P); GAPA(5, C, P); GAPA(6, C, P); GAPA(7, C, P); \
        lsum += sacc; \
        PVM(0); PVM(1); SBAR(); \
        bool resc = false; float alpha = 1.f, lold = lsum; \
        if (MODE == 0) { \
          const float rm = rowmax16(C);                          \
          if (__builtin_expect(__any(rm > THR), 0)) { const float dl = fmaxf(rm, 0.f); alpha = __builtin_amdgcn_exp2f(-dl); lsum *= alpha; resc = true; \
            const float nm_ = negm[0] - dl; SETM(nm_); _Pragma("unroll") for (int r = 0; r < 16; ++r) C[r] -= dl; } \
        } else if (MODE == 1) { const float m0 = rowmax16(C); SETM(-m0); lsum = 0.f; _Pragma("unroll") for (int r = 0; r < 16; ++r) C[r] -= m0; } \
        SBAR(); \
        if (MODE != 2) { \
          KRD(0); KRD(1); PVM(2); C[0] = EX(C[0]); C[1] = EX(C[1]); C[2] = EX(C[2]); PIN(C); SBAR(); \
          KRD(2); PVM(3); C[3] = EX(C[3]); C[4] = EX(C[4]); C[5] = EX(C[5]); PIN(C); SBAR(); \
          KRD(3); KRD(4); PVM(4); C[6] = EX(C[6]); C[7] = EX(C[7]); C[8] = EX(C[8]); PIN(C); SBAR(); \
          KRD(5); PVM(5); C[9] = EX(C[9]); C[10] = EX(C[10]); C[11] = EX(C[11]); PIN(C); SBAR(); \
          KRD(6); PVM(6); C[12] = EX(C[12]); C[13] = EX(C[13]); PIN(C); SBAR(); \
          KRD(7); PVM(7); C[14] = EX(C[14]); C[15] = EX(C[15]); PIN(C); SBAR(); \
        } else { PVM(2); PVM(3); PVM(4); PVM(5); PVM(6); PVM(7); } \
        if (MODE == 0) { if (resc) { _Pragma("unroll") for (int dt = 0; dt < 4; ++dt) _Pragma("unroll") for (int r = 0; r < 16; ++r) o[dt][r] *= alpha; } } \
        else {   \
          auto rr = __builtin_amdgcn_permlane32_swap(__float_as_uint(lold), __float_as_uint(lold), false, false); \
          const float inv = 1.0f / (__uint_as_float(rr[0]) + __uint_as_float(rr[1])); \
          _Pragma("unroll") for (int dt = 0; dt < 4; ++dt) { st128(qo + (2 * dt) * 1024, pack8s(o[dt], 0, inv)); st128(qo + (2 * dt + 1) * 1024, pack8s(o[dt], 1, inv)); o[dt] = f32x16{}; } \
        } \
        BAR(); } while (0)
      for (int t = 0; t < 7; ++t) { STEP(pB, pA, 0); STEP(pA, pB, 0); }
      STEP(pB, pA, 0);
      for (int u = 1; u < 4; ++u) {
        const lds_cptr qsrc = (u == 1) ? (lds + Q1_OFF + qb * 8192 + lane * 16) : (lds_cptr)qo;
#pragma unroll
        for (int s = 0; s < 8; ++s) qf[s] = ld128(qsrc + s * 1024);
        asm volatile("s_waitcnt lgkmcnt(0)" ::: "memory");
        STEP(pA, pB, 1);
        if (w == 0) STAMP(1 + u);
        for (int t = 0; t < 7; ++t) { STEP(pB, pA, 0); STEP(pA, pB, 0); }
        STEP(pB, pA, 0);
      }
      STEP(pA, pB, 2);
      if (w == 0) STAMP(5);
#undef STEP
#undef PVM
#undef GAPA
#undef VRD
#undef KRD
#undef EX
#undef SETM
      BAR();
    }
  } else if (w == 3) {
    bf16x8 q2[2][3][8];
    {
      f32x16 acc[4][3];
      qphase(acc, 2);
#pragma unroll
      for (int q = 0; q < 3; ++q)
#pragma unroll
        for (int d = 0; d < 4; ++d) { q2[0][q][2 * d] = asfrag(pack8s(acc[d][q], 0, C_SCALE)); q2[0][q][2 * d + 1] = asfrag(pack8s(acc[d][q], 1, C_SCALE)); }
      BAR();
    }
    {
      LANE_CONSTS();
      const u16* kg = br ? kB : kA; const u16* vg = br ? vB : vA; const u16* wog = br ? woB : woA;
      const size_t kvbase = ((size_t)b * 32 + 4 * g) * (512 * 128);
      const __amdgpu_buffer_rsrc_t srdK = __builtin_amdgcn_make_buffer_rsrc((void*)(kg + kvbase), 0, 4 * 512 * 128 * 2, 0x00020000);
      const __amdgpu_buffer_rsrc_t srdV = __builtin_amdgcn_make_buffer_rsrc((void*)(vg + kvbase), 0, 4 * 512 * 128 * 2, 0x00020000);
      const __amdgpu_buffer_rsrc_t srdWo = __builtin_amdgcn_make_buffer_rsrc((void*)(wog + (size_t)g * 512 * 512), 0, 512 * 512 * 2, 0x00020000);
      const unsigned voffV = r8 * 256u + 64u * h + 16u * slot4;
      const unsigned voffK0 = r8 * 256u + 64u * h + 16u * (slot4 ^ (r8 >> 2)), voffK1 = voffK0 ^ 32u;
      const unsigned voffWo = r8 * 1024u + 64u * h + 16u * slot4;
      auto issue_kv = [&](int T, int slot) {
        const unsigned so = (((unsigned)T & ~15u) | (((unsigned)T + krot) & 15u)) * 8192u, dst = lds0 + slot;
#pragma unroll
        for (int i = 0; i < 8; ++i) dma16nt(srdK, ((i >> 1) & 1) ? voffK1 : voffK0, so + 2048u * (i >> 1) + 128u * (i & 1), dst + 1024u * i);
#pragma unroll
        for (int i = 0; i < 8; ++i) dma16nt(srdV, voffV, so + 2048u * (i >> 1) + 128u * (i & 1), dst + 8192u + 1024u * i);
      };
      auto issue_wo16 = [&](int r, int slot) {
        const unsigned rr = (r + (b >> 1)) & 7;
        const unsigned so = (384u + 16u * rr) * 1024u, dst = lds0 + slot;
#pragma unroll
        for (int i = 0; i < 16; ++i) dma16(srdWo, voffWo, so + 512u * (i >> 3) + 8192u * ((i >> 2) & 1) + 128u * (i & 3), dst + 1024u * i);
      };
      {
        const lds_cptr h0 = lds + WO_OFF + lane * 16, h2 = lds + R3B_OFF + lane * 16;
#pragma unroll
        for (int s = 0; s < 8; ++s) { q2[1][0][s] = ld128(h0 + s * 1024); q2[1][1][s] = ld128(h0 + 8192 + s * 1024); q2[1][2][s] = ld128(h2 + s * 1024); }
      }
      BAR();
      BAR();
      int islot = KV_FIRST;
      const lds_ptr qo = lds + QO_OFF + lane * 16;
      for (int T = 2; T < 60; ++T) {
        issue_kv(T + 4, islot);
        islot = (islot == KV_END - KV_SLOT) ? KV_OFF : islot + KV_SLOT;
        if (T == 31) {
#pragma unroll
          for (int q = 0; q < 3; ++q)
#pragma unroll
            for (int s = 0; s < 8; ++s) st128(qo + q * 8192 + s * 1024, __builtin_bit_cast(u32x4, q2[0][q][s]));
        }
        if (T == 47) {
#pragma unroll
          for (int q = 0; q < 3; ++q)
#pragma unroll
            for (int s = 0; s < 8; ++s) st128(qo + q * 8192 + s * 1024, __builtin_bit_cast(u32x4, q2[1][q][s]));
        }
        WAITVM(32);
        BAR();
      }
      for (int r = 0; r < 5; ++r) {
        issue_wo16(r, islot);
        islot = (islot == KV_END - KV_SLOT) ? KV_OFF : islot + KV_SLOT;
        if (r < 4) WAITVM(32); else WAITVM(0);
        BAR();
      }
      issue_wo16(6, islot);
      WAITVM(0);
      BAR();
    }
  } else {
    const int pw = w - 4;
    const u16* wog = br ? woB : woA;
    {
      LANE_CONSTS();
      const u16* xg = br ? xB : xA; const u16* wqg = br ? wqB : wqA; const u16* kg = br ? kB : kA; const u16* vg = br ? vB : vA;
      const size_t kvbase = ((size_t)b * 32 + 4 * g) * (512 * 128);
      const __amdgpu_buffer_rsrc_t srdWq = __builtin_amdgcn_make_buffer_rsrc((void*)(wqg + g * 512), 0, 512 * 4096 * 2 - g * 1024, 0x00020000);
      const __amdgpu_buffer_rsrc_t srdX = __builtin_amdgcn_make_buffer_rsrc((void*)(xg + (size_t)b * 96 * 512), 0, 96 * 512 * 2, 0x00020000);
      const __amdgpu_buffer_rsrc_t srdK = __builtin_amdgcn_make_buffer_rsrc((void*)(kg + kvbase), 0, 4 * 512 * 128 * 2, 0x00020000);
      const __amdgpu_buffer_rsrc_t srdV = __builtin_amdgcn_make_buffer_rsrc((void*)(vg + kvbase), 0, 4 * 512 * 128 * 2, 0x00020000);
      const unsigned voffWq = r8 * 8192u + 64u * h + 16u * slot4;
      const unsigned voffX0 = r8 * 1024u + 64u * h + 16u * (slot4 ^ (r8 >> 2)), voffX1 = voffX0 ^ 32u;
      const unsigned voffK = (8u * pw + r8) * 256u + 64u * h + 16u * (slot4 ^ (r8 >> 2) ^ (2u * (pw & 1)));
      const unsigned voffV = (8u * pw + r8) * 256u + 64u * h + 16u * slot4;
      const unsigned rot = 2u * (b & 7);
      auto issue_w = [&](int j, int st) {
        const unsigned jj = (j + rot) & 15;
        const unsigned so = jj * (32u * 8192u) + pw * 256u, dst = lds0 + st + 8192u * pw;
#pragma unroll
        for (int i = 0; i < 8; ++i) dma16nt(srdWq, voffWq, so + 65536u * (i >> 1) + 128u * (i & 1), dst + 1024u * i);
      };
      auto issue_x = [&](int j) {
        const unsigned jj = (j + rot) & 15;
        const unsigned dst = lds0 + ((j & 2) ? XS_1 : XS_0) + 3072u * pw;
#pragma unroll
        for (int i = 0; i < 3; ++i) dma16nt(srdX, ((3 * pw + i) & 1) ? voffX1 : voffX0, jj * 64u + (3u * pw + i) * 8192u, dst + 1024u * i);
      };
      auto issue_t = [&](int T) {
        const unsigned so = (((unsigned)T + krot) & 15u) * 8192u, dst = lds0 + KV_OFF + ((T + 4) % 6) * KV_SLOT + 2048u * pw;
        dma16nt(srdK, voffK, so, dst); dma16nt(srdK, voffK, so + 128u, dst + 1024u);
        dma16nt(srdV, voffV, so, dst + 8192u); dma16nt(srdV, voffV, so + 128u, dst + 8192u + 1024u);
      };
      auto nextst = [](int st) { return (st == WS_A) ? WS_C : (st == WS_C) ? WS_B : WS_A; };
      issue_x(0);
      issue_w(0, WS_A);
      issue_w(1, WS_C);
      issue_t(0);
      issue_t(1);
      WAITVM(16);
      BAR();
      int st2 = WS_B;
      issue_x(2); issue_w(2, st2); st2 = nextst(st2); WAITVM(19); BAR();
      for (int j = 1; j < 13; j += 2) {
        issue_w(j + 2, st2); st2 = nextst(st2); WAITVM(8); BAR();
        issue_x(j + 3); issue_w(j + 3, st2); st2 = nextst(st2); WAITVM(11); BAR();
      }
      issue_w(15, st2); WAITVM(8); BAR();
      issue_t(2); WAITVM(4); BAR();
      issue_t(3); issue_t(4); WAITVM(12); BAR();
      BAR();
      WAITVM(8); BAR();
      issue_t(5); WAITVM(8); BAR();
      WAITVM(4); BAR();
      WAITVM(0); BAR();
      for (int T = 4; T < 15; ++T) BAR();
    }
    LANE_CONSTS();
    const __amdgpu_buffer_rsrc_t srdWo = __builtin_amdgcn_make_buffer_rsrc((void*)(wog + (size_t)g * 512 * 512), 0, 512 * 512 * 2, 0x00020000);
    const unsigned voffWo = (8u * (pw >> 1) + r8) * 1024u + 64u * (4u * (pw & 1) + h) + 16u * slot4;
    auto issue_wo = [&](int u, int m, int slot) {
      const unsigned rr = ((m >> 1) + (b >> 1)) & 7;
      const unsigned so = (128u * u + 16u * rr) * 1024u + ((m ^ b) & 1) * 512u;
      const unsigned dst = lds0 + WO_OFF + slot + 2048u * pw;
      dma16(srdWo, voffWo, so, dst);
      dma16(srdWo, voffWo, so + 128u, dst + 1024u);
    };
    const unsigned ob0 = 256u * h + 64u * q4 + 32u * blk + 16u * (p & 1) + 8u * (p >> 1) + 1024u * pw;
    const lds_cptr qo = lds + QO_OFF + lane * 16;
    f32x16 acc[2][2][3];
#pragma unroll
    for (int a = 0; a < 2; ++a)
#pragma unroll
      for (int c = 0; c < 2; ++c)
#pragma unroll
        for (int qb = 0; qb < 3; ++qb) acc[a][c][qb] = f32x16{};
    bf16x8 of[3], ofn[3];
    auto issue_M = [&](int M, int slot) { if (M < 48) issue_wo(M >> 4, M & 15, slot); else issue_wo(3, 10 + (M & 1) + 2 * (M & 2), slot); };
    issue_wo(0, 0, 0 * WO_SLOT);  BAR();
    issue_wo(0, 1, 1 * WO_SLOT);  WAITVM(2); BAR();
    issue_wo(0, 2, 2 * WO_SLOT); issue_wo(0, 3, 3 * WO_SLOT);
    int cur = 0, nxt = 4 * WO_SLOT;
    auto rot = [&]() { cur = (cur == 4 * WO_SLOT) ? 0 : cur + WO_SLOT; nxt = (nxt == 4 * WO_SLOT) ? 0 : nxt + WO_SLOT; };
    const int rb = b >> 1;
    for (int M = 0; M < 48; M += 2) {
      const int m = M & 15;
      {
        issue_M(M + 4, nxt);
        if (m == 0) {
#pragma unroll
          for (int qb = 0; qb < 3; ++qb) of[qb] = ld128(qo + qb * 8192 + (rb & 7) * 1024);
        }
        const lds_cptr wp = lds + WO_OFF + cur + ob0;
#pragma unroll
        for (int c = 0; c < 2; ++c) {
          bf16x8 wf = ldtr2(wp + 512 * c, 4096);
#pragma unroll
          for (int qb = 0; qb < PMN; ++qb) acc[0][c][qb] = MFMA(wf, of[qb], acc[0][c][qb]);
        }
        WAITVM(6);
        BAR();
        rot();
      }
      {
        issue_M(M + 5, nxt);
        if (m != 14) {
#pragma unroll
          for (int qb = 0; qb < 3; ++qb) ofn[qb] = ld128(qo + qb * 8192 + (((m >> 1) + 1 + rb) & 7) * 1024);
        }
        const lds_cptr wp = lds + WO_OFF + cur + ob0;
#pragma unroll
        for (int c = 0; c < 2; ++c) {
          bf16x8 wf = ldtr2(wp + 512 * c, 4096);
#pragma unroll
          for (int qb = 0; qb < PMN; ++qb) acc[1][c][qb] = MFMA(wf, of[qb], acc[1][c][qb]);
        }
#pragma unroll
        for (int qb = 0; qb < 3; ++qb) of[qb] = ofn[qb];
        if (M < 46) WAITVM(6); else WAITVM(0);
        BAR();
        rot();
      }
    }
    if (pw == 0) STAMP(7);
    {
      const unsigned hsel = (b & 1) * 8192u;
      auto range = [&](int r, lds_cptr wp0, lds_cptr wp1) __attribute__((always_inline)) {
#pragma unroll
        for (int qb = 0; qb < 3; ++qb) of[qb] = ld128(qo + qb * 8192 + ((r + rb) & 7) * 1024);
#pragma unroll
        for (int c = 0; c < 2; ++c) {
          const bf16x8 wf0 = ldtr2(wp0 + 512 * c, 4096), wf1 = ldtr2(wp1 + 512 * c, 4096);
#pragma unroll
          for (int qb = 0; qb < 3; ++qb) { acc[0][c][qb] = MFMA(wf0, of[qb], acc[0][c][qb]); acc[1][c][qb] = MFMA(wf1, of[qb], acc[1][c][qb]); }
        }
      };
      int sl = KV_OFF + ((58 + 4) % 6) * KV_SLOT;
      for (int r = 0; r < 5; ++r) {
        range(r, lds + sl + ob0 + hsel, lds + sl + ob0 + (hsel ^ 8192u));
        sl = (sl == KV_END - KV_SLOT) ? KV_OFF : sl + KV_SLOT;
      }
      auto nx = [](int s) { return (s == 4 * WO_SLOT) ? 0 : s + WO_SLOT; };
      const int c1 = nx(cur), c2 = nx(c1), c3 = nx(c2);
      range(5, lds + WO_OFF + cur + ob0, lds + WO_OFF + c1 + ob0);
      range(7, lds + WO_OFF + c2 + ob0, lds + WO_OFF + c3 + ob0);
      BAR();
      range(6, lds + sl + ob0 + hsel, lds + sl + ob0 + (hsel ^ 8192u));
    }
    const __amdgpu_buffer_rsrc_t srdS = __builtin_amdgcn_make_buffer_rsrc((void*)(ws + (size_t)((br * 16 + b) * 8 + g) * SLAB_BYTES), 0, (int)SLAB_BYTES, 0x00020000);
#pragma unroll
    for (int a = 0; a < 2; ++a)
#pragma unroll
      for (int qb = 0; qb < 3; ++qb)
#pragma unroll
        for (int c = 0; c < 2; ++c)
#pragma unroll
          for (int s = 0; s < 2; ++s)
            __builtin_amdgcn_raw_buffer_store_b128(pack8n(acc[a][c][qb], s), srdS, lane * 16, (4 * (a ^ (b & 1)) + pw) * 12288 + ((qb * 2 + c) * 2 + s) * 1024, 16);
    asm volatile("s_waitcnt vmcnt(0)" ::: "memory");
    if (pw == 0) STAMP(8);
  }
  asm volatile("s_waitcnt lgkmcnt(0)\n\ts_barrier" ::: "memory");
  LANE_CONSTS();
  if (w == 0) {
    const unsigned long long want = (((unsigned long long)gen_hi << 32) | gen_lo) + 1;
    if (lane == 0) __hip_atomic_store(flagp + 16 + g, want, __ATOMIC_RELAXED, __HIP_MEMORY_SCOPE_AGENT);
    for (unsigned spins = 0; spins < (1u << 22); ++spins) {
      const unsigned long long f = __hip_atomic_load(flagp + 16 + (lane & 7), __ATOMIC_RELAXED, __HIP_MEMORY_SCOPE_AGENT);
      if (__all(f == want)) break;
      __builtin_amdgcn_s_sleep(1);
    }
    if (g == 0 && lane == 0) __hip_atomic_store(flagp, want, __ATOMIC_RELAXED, __HIP_MEMORY_SCOPE_AGENT);
    STAMP(9);
  }
  asm volatile("s_waitcnt lgkmcnt(0)\n\ts_barrier" ::: "memory");
  {
    const __amdgpu_buffer_rsrc_t srdG = __builtin_amdgcn_make_buffer_rsrc((void*)(ws + (size_t)((br * 16 + b) * 8) * SLAB_BYTES), 0, (int)(8 * SLAB_BYTES), 0x00020000);
    const int nch = (w < 4) ? 2 : 1;
    u32x4 v[2][8];
#pragma unroll
    for (int i = 0; i < 2; ++i)
      if (i < nch) {
#pragma unroll
        for (int s = 0; s < 8; ++s) v[i][s] = __builtin_amdgcn_raw_buffer_load_b128(srdG, lane * 16, s * (int)SLAB_BYTES + g * 12288 + (w + 8 * i) * 1024, 16);
      }
#pragma unroll
    for (int i = 0; i < 2; ++i)
      if (i < nch) {
        float a[8] = {0.f, 0.f, 0.f, 0.f, 0.f, 0.f, 0.f, 0.f};
#pragma unroll
        for (int s = 0; s < 8; ++s)
#pragma unroll
          for (int j = 0; j < 4; ++j) { a[2 * j] += __uint_as_float(v[i][s][j] << 16); a[2 * j + 1] += __uint_as_float(v[i][s][j] & 0xffff0000u); }
        const u32x4 r = {cvtpk(a[0], a[1]), cvtpk(a[2], a[3]), cvtpk(a[4], a[5]), cvtpk(a[6], a[7])};
        const int f = w + 8 * i, qb = f >> 2, c = (f >> 1) & 1, s = f & 1;
        const int q = 32 * qb + l32, n = 64 * g + 32 * c + 16 * s + 8 * h;
        *(u32x4*)(out + ((size_t)(b * 96 + q)) * 1024 + br * 512 + n) = r;
      }
  }
}

extern "C" void kernel_launch(void* const* d_in, const int* in_sizes, int n_in, void* d_out, int out_size, void* d_ws, size_t ws_size, hipStream_t stream) {
  (void)in_sizes; (void)n_in; (void)out_size; (void)ws_size;
  (void)hipFuncSetAttribute((const void*)fused_attn, hipFuncAttributeMaxDynamicSharedMemorySize, LDS_BYTES);
  hipLaunchKernelGGL(fused_attn, dim3(256), dim3(512), LDS_BYTES, stream,
                     (const u16*)d_in[0], (const u16*)d_in[1], (const u16*)d_in[2], (const u16*)d_in[3], (const u16*)d_in[4],
                     (const u16*)d_in[5], (const u16*)d_in[6], (const u16*)d_in[7], (const u16*)d_in[8], (const u16*)d_in[9], (char*)d_ws, (u16*)d_out
  );
}
```

```cpp
#include <hip/hip_runtime.h>
#include <stdint.h>

typedef unsigned short u16;
typedef short bf16x8 __attribute__((ext_vector_type(8)));
typedef short s16x4 __attribute__((ext_vector_type(4)));
typedef float f32x16 __attribute__((ext_vector_type(16)));
typedef float f32x2 __attribute__((ext_vector_type(2)));
typedef __bf16 bf16x2 __attribute__((ext_vector_type(2)));
typedef unsigned u32x4 __attribute__((ext_vector_type(4)));
#define LAS __attribute__((address_space(3)))
#define DI __device__ __forceinline__
typedef LAS const char* lds_cptr;
typedef LAS char* lds_ptr;
typedef __attribute__((address_space(1))) unsigned long long gu64;

constexpr int QO_OFF = 0;
constexpr int WO_OFF = 24576, WO_SLOT = 8192, WO_END = 65536;
constexpr int Q1_OFF = 40960;
constexpr int KV_OFF = 65536, KV_SLOT = 16384, KV_END = 163840;
constexpr int KV_FIRST = KV_OFF + 4 * KV_SLOT;
constexpr int WS_A = 0, WS_C = 49152, WS_B = 81920;
constexpr int XS_0 = 32768, XS_1 = 114688;
constexpr int kvhalf(int tile, int v) { return KV_OFF + ((tile + 4) % 6) * KV_SLOT + 8192 * v; }
constexpr int TAILC(int i) {
  return i == 0 ? kvhalf(58, 0) : i == 1 ? kvhalf(58, 1) : i == 2 ? kvhalf(59, 0) : i == 3 ? kvhalf(60, 0) : i == 4 ? kvhalf(59, 1) : i == 5 ? kvhalf(61, 0)
       : i == 6 ? kvhalf(60, 1) : i == 7 ? kvhalf(62, 0) : i == 8 ? kvhalf(61, 1) : i == 9 ? kvhalf(63, 0)
       : i == 10 ? WO_OFF + 3 * WO_SLOT : i == 11 ? WO_OFF + 4 * WO_SLOT : i == 12 ? WO_OFF + 0 * WO_SLOT : i == 13 ? kvhalf(62, 1) : i == 14 ? WO_OFF + 1 * WO_SLOT : kvhalf(63, 1);
}
constexpr int RDY_OFF = WO_OFF + 2 * WO_SLOT;
constexpr int R3B_OFF = 114688;
constexpr int LDS_BYTES = 163840;
constexpr size_t SLAB_BYTES = 98304;
constexpr size_t FLAG_OFF = 32u << 20;
constexpr float C_SCALE = 0.08838834764831845f * 1.4426950408889634f;
constexpr float THR = 6.0f;

#define PMN 3
#define WO_DFLT
#define PACE 40
#define NOPN "3"
#define SBAR() __builtin_amdgcn_sched_barrier(0)
#define PIN(x) asm volatile("" : "+v"(x))
#define MFMA(a, b, c) __builtin_amdgcn_mfma_f32_32x32x16_bf16((a), (b), (c), 0, 0, 0)

DI unsigned cvtpk(float lo, float hi) { f32x2 v = {lo, hi}; bf16x2 b = __builtin_convertvector(v, bf16x2); return __builtin_bit_cast(unsigned, b); }
DI bf16x8 ld128(lds_cptr p) { return *(const LAS bf16x8*)p; }
DI void st128(lds_ptr p, u32x4 v) { *(LAS u32x4*)p = v; }
DI s16x4 ldtr(lds_cptr p) { typedef short v4i16_t __attribute__((ext_vector_type(4))); return __builtin_bit_cast(s16x4, __builtin_amdgcn_ds_read_tr16_b64_v4i16((LAS v4i16_t*)p)); }
DI bf16x8 cat(s16x4 lo, s16x4 hi) { return __builtin_shufflevector(lo, hi, 0, 1, 2, 3, 4, 5, 6, 7); }
DI bf16x8 ldtr2(lds_cptr p, int second) { return cat(ldtr(p), ldtr(p + second)); }
DI u32x4 pack8s(const f32x16& x, int s, float sc) {
  u32x4 w = {cvtpk(x[8 * s] * sc, x[8 * s + 1] * sc), cvtpk(x[8 * s + 2] * sc, x[8 * s + 3] * sc), cvtpk(x[8 * s + 4] * sc, x[8 * s + 5] * sc), cvtpk(x[8 * s + 6] * sc, x[8 * s + 7] * sc)};
  return w;
}
DI u32x4 pack8n(const f32x16& x, int s) {
  u32x4 w = {cvtpk(x[8 * s], x[8 * s + 1]), cvtpk(x[8 * s + 2], x[8 * s + 3]), cvtpk(x[8 * s + 4], x[8 * s + 5]), cvtpk(x[8 * s + 6], x[8 * s + 7])};
  return w;
}
DI bf16x8 asfrag(u32x4 v) { return __builtin_bit_cast(bf16x8, v); }
DI void dma16(__amdgpu_buffer_rsrc_t srd, unsigned voff, unsigned soff, unsigned ldsaddr) {
  asm volatile("s_mov_b32 m0, %0\n\ts_nop " NOPN "\n\tbuffer_load_dwordx4 %1, %2, %3 offen lds" ::"s"(ldsaddr), "v"(voff), "s"(srd), "s"(soff) : "memory");
}
DI void dma16nt(__amdgpu_buffer_rsrc_t srd, unsigned voff, unsigned soff, unsigned ldsaddr) {
  asm volatile("s_mov_b32 m0, %0\n\ts_nop " NOPN "\n\tbuffer_load_dwordx4 %1, %2, %3 offen nt lds" ::"s"(ldsaddr), "v"(voff), "s"(srd), "s"(soff) : "memory");
}
DI int fresh_lane() { int l; asm volatile("v_mbcnt_lo_u32_b32 %0, -1, 0\n\tv_mbcnt_hi_u32_b32 %0, -1, %0" : "=v"(l)); return l; }
#define LANE_CONSTS() const int lane = fresh_lane(); const int l32 = lane & 31, h = lane >> 5; const int blk = (lane >> 4) & 1, q4 = (lane & 15) >> 2, p = lane & 3; \
  const unsigned r8 = (lane >> 2) & 7, slot4 = lane & 3; (void)l32; (void)h; (void)blk; (void)q4; (void)p; (void)r8; (void)slot4
#define WAITVM(n) asm volatile("s_waitcnt vmcnt(" #n ")" ::: "memory")

#define STAMP(i)
#define BAR() do { __builtin_amdgcn_sched_barrier(0); __builtin_amdgcn_s_waitcnt(0xC07F); asm volatile("s_barrier" ::: "memory"); } while (0)

DI float rowmax16(const f32x16& c) {
  float a = fmaxf(fmaxf(c[0], c[1]), c[2]), b = fmaxf(fmaxf(c[3], c[4]), c[5]);
  a = fmaxf(fmaxf(a, c[6]), c[7]); b = fmaxf(fmaxf(b, c[8]), c[9]);
  a = fmaxf(fmaxf(a, c[10]), c[11]); b = fmaxf(fmaxf(b, c[12]), c[13]);
  a = fmaxf(fmaxf(a, c[14]), c[15]);
  const float m = fmaxf(a, b);
  auto rr = __builtin_amdgcn_permlane32_swap(__float_as_uint(m), __float_as_uint(m), false, false);
  return fmaxf(__uint_as_float(rr[0]), __uint_as_float(rr[1]));
}

__global__ __launch_bounds__(512, 2) void fused_attn(const u16* __restrict__ xA, const u16* __restrict__ kA, const u16* __restrict__ vA, const u16* __restrict__ wqA,
                                                   const u16* __restrict__ woA, const u16* __restrict__ xB, const u16* __restrict__ kB, const u16* __restrict__ vB,
                                                   const u16* __restrict__ wqB, const u16* __restrict__ woB, char* __restrict__ ws, u16* __restrict__ out
) {
  extern __shared__ __attribute__((aligned(16))) char smem[];
  const lds_ptr lds = (lds_ptr)smem;
  const unsigned lds0 = (unsigned)(uintptr_t)smem;
  const int w = __builtin_amdgcn_readfirstlane(threadIdx.x >> 6);
  const int bid = blockIdx.x, xcd = bid & 7, idx = bid >> 3;
  const int br = xcd >> 2, g = 2 * (xcd & 3) + (idx & 1), b = idx >> 1;
  const unsigned krot = (idx + 2 * xcd) & 15;
  gu64* const flagp = (gu64*)(ws + FLAG_OFF + (size_t)(br * 16 + b) * 256);
  unsigned gen_lo = 0, gen_hi = 0;

  auto qphase = [&](f32x16 (&acc)[4][3], int head) __attribute__((always_inline)) {
    LANE_CONSTS();
    const unsigned wqb = 8192u * head + 2048u * h + 64u * q4 + 32u * blk + 16u * (p & 1) + 8u * (p >> 1);
    const unsigned xb0 = 1024u * (l32 >> 3) + 64u * (l32 & 7) + 16u * (h ^ ((l32 >> 2) & 3));
#pragma unroll
    for (int d = 0; d < 4; ++d)
#pragma unroll
      for (int q = 0; q < 3; ++q) acc[d][q] = f32x16{};
    bf16x8 wa[2], wb[2], x0[3], x1[3];
    if (w == 0) STAMP(13);
    BAR();
    if (w == 0) STAMP(12);
#define QM(W, X, D0, i) acc[D0 + (i) / 3][(i) % 3] = MFMA(W[(i) / 3], X[(i) % 3], acc[D0 + (i) / 3][(i) % 3])
#define QW(KS, D) ldtr2(wp + 4096 * (KS) + 512 * (D), 256)
#define QMM(W, X, D0) do { QM(W, X, D0, 0); QM(W, X, D0, 1); QM(W, X, D0, 2); QM(W, X, D0, 3); QM(W, X, D0, 4); QM(W, X, D0, 5); SBAR(); } while (0)
#define QSTEP(j, FIRST) do { \
      const lds_cptr wp = lds + st + wqb; \
      const int xs = (((j) & 2) ? XS_1 : XS_0) + 512 * ((j) & 1); \
      const lds_cptr xp0 = lds + xs + xb0, xp1 = lds + xs + (xb0 ^ 32u); \
      if (FIRST) { x0[0] = ld128(xp0); x0[1] = ld128(xp0 + 4096); x0[2] = ld128(xp0 + 8192); wa[0] = QW(0, 0); wa[1] = QW(0, 1); SBAR(); } \
      else { \
        QM(wb, x1, 2, 0); x0[0] = ld128(xp0); x0[1] = ld128(xp0 + 4096); SBAR(); \
        QM(wb, x1, 2, 1); x0[2] = ld128(xp0 + 8192); wa[0] = QW(0, 0); SBAR(); \
        QM(wb, x1, 2, 2); wa[1] = QW(0, 1); SBAR(); \
        QM(wb, x1, 2, 3); QM(wb, x1, 2, 4); QM(wb, x1, 2, 5); SBAR(); } \
      QM(wa, x0, 0, 0); wb[0] = QW(0, 2); SBAR(); \
      QM(wa, x0, 0, 1); wb[1] = QW(0, 3); SBAR(); \
      QM(wa, x0, 0, 2); x1[0] = ld128(xp1); x1[1] = ld128(xp1 + 4096); SBAR(); \
      QM(wa, x0, 0, 3); x1[2] = ld128(xp1 + 8192); SBAR(); \
      QM(wa, x0, 0, 4); QM(wa, x0, 0, 5); SBAR(); \
      QM(wb, x0, 2, 0); wa[0] = QW(1, 0); SBAR(); \
      QM(wb, x0, 2, 1); wa[1] = QW(1, 1); SBAR(); \
      QM(wb, x0, 2, 2); QM(wb, x0, 2, 3); QM(wb, x0, 2, 4); QM(wb, x0, 2, 5); SBAR(); \
      QM(wa, x1, 0, 0); wb[0] = QW(1, 2); SBAR(); \
      QM(wa, x1, 0, 1); wb[1] = QW(1, 3); SBAR(); \
      QM(wa, x1, 0, 2); QM(wa, x1, 0, 3); QM(wa, x1, 0, 4); QM(wa, x1, 0, 5); \
      st = (st == WS_A) ? WS_C : (st == WS_C) ? WS_B : WS_A; \
      BAR(); } while (0)
    int st = WS_A;
    QSTEP(0, true);
#pragma unroll 1
    for (int j = 1; j < 16; ++j) {
      QSTEP(j, false);
    }
#undef QSTEP
    QMM(wb, x1, 2);
#undef QMM
#undef QM
#undef QW
  };

  if (w < 3) {
    {
      { const int lane = fresh_lane(); (void)lane;
      if (w == 0) { STAMP(0); const unsigned long long g0 = __hip_atomic_load(flagp, __ATOMIC_RELAXED, __HIP_MEMORY_SCOPE_AGENT);
          gen_lo = __builtin_amdgcn_readfirstlane((unsigned)g0); gen_hi = __builtin_amdgcn_readfirstlane((unsigned)(g0 >> 32)); } }
      f32x16 acc[4][3];
      qphase(acc, (w < 2) ? w : 3);
      const int lane = fresh_lane();
      const int o0 = (w == 0) ? QO_OFF : (w == 1) ? Q1_OFF : WO_OFF, o2 = (w == 0) ? QO_OFF + 16384 : (w == 1) ? Q1_OFF + 16384 : R3B_OFF;
#pragma unroll
      for (int q = 0; q < 3; ++q) {
        const lds_ptr dst = lds + ((q < 2) ? o0 + q * 8192 : o2) + lane * 16;
#pragma unroll
        for (int d = 0; d < 4; ++d) { st128(dst + (2 * d) * 1024, pack8s(acc[d][q], 0, C_SCALE)); st128(dst + (2 * d + 1) * 1024, pack8s(acc[d][q], 1, C_SCALE)); }
      }
      BAR();
    }
    {
      const int qb = w;
      LANE_CONSTS();
      if (w == 0) STAMP(1);
      __builtin_amdgcn_s_setprio(3);
      const unsigned kb0 = 2048u * (l32 >> 3) + 64u * (l32 & 7) + 16u * (h ^ ((l32 >> 2) & 3));
      const unsigned vb0 = 8192u + 256u * h + 64u * q4 + 32u * blk + 8u * p;
      const lds_ptr qo = lds + QO_OFF + qb * 8192 + lane * 16;
      f32x16 o[4], pA, pB, negm;
      bf16x8 qf[8], kf[8], vf[8];
      u32x4 pw0, pw1;
      float lsum = 0.f;
      int kvp = KV_FIRST, kvc = KV_FIRST;
      auto nextslot = [](int s) { return (s == KV_END - KV_SLOT) ? KV_OFF : s + KV_SLOT; };
#define EX(v) __builtin_amdgcn_exp2f(v)
#define SETM(x) do { _Pragma("unroll") for (int r_ = 0; r_ < 16; ++r_) negm[r_] = (x); PIN(negm); } while (0)
#define KRD_(i) kf[i] = ld128(((i & 1) ? kp1 : kp0) + 512 * (i >> 1))
#define KRD(i) do { if (MODE != 1) KRD_(i); } while (0)
#define VRD(i) vf[i] = ldtr2(vp + 4096 * ((i) >> 2) + 512 * ((i) & 3), 2048)
      {
#pragma unroll
        for (int s = 0; s < 8; ++s) qf[s] = ld128(qo + s * 1024);
        { const lds_cptr kp0 = lds + kvc + kb0, kp1 = lds + kvc + (kb0 ^ 32u);
#pragma unroll
          for (int s = 0; s < 8; ++s) KRD_(s); }
        pA = MFMA(kf[0], qf[0], f32x16{});
#pragma unroll
        for (int s = 1; s < 8; ++s) pA = MFMA(kf[s], qf[s], pA);
        SBAR();
        kvc = nextslot(kvc);
        { const lds_cptr kp0 = lds + kvc + kb0, kp1 = lds + kvc + (kb0 ^ 32u);
#pragma unroll
          for (int s = 0; s < 8; ++s) KRD_(s); }
        const float m0 = rowmax16(pA);
        SETM(-m0);
#pragma unroll
        for (int r = 0; r < 16; ++r) pA[r] = EX(pA[r] - m0);
        BAR();
      }
#define GAPA(i, C, P) do { VRD(i); if (MODE != 2) { if (i == 0) C = MFMA(kf[0], qf[0], MODE == 0 ? negm : f32x16{}); else C = MFMA(kf[i], qf[i], C); } \
        if (i < 7) { sacc += P[2 * i + 2]; sacc += P[2 * i + 3]; } \
        if (i < 4) pw0[i] = cvtpk(P[2 * i], P[2 * i + 1]); else pw1[i - 4] = cvtpk(P[2 * i], P[2 * i + 1]); \
        PIN(sacc); if (i < 4) PIN(pw0); else PIN(pw1); SBAR(); } while (0)
#define PVM(i) o[(i) & 3] = MFMA(vf[i], asfrag(((i) < 4) ? pw0 : pw1), (ZC && (i) < 4) ? f32x16{} : o[(i) & 3])
#define QRD(i) do { if (QPF) qf[i] = ld128(qsrc + (i) * 1024); } while (0)
#define STEP(C, P, MODE_, ZC_, QPF_) do { constexpr int MODE = MODE_; constexpr bool ZC = ZC_, QPF = QPF_; \
        const lds_cptr vp = lds + kvp + vb0; \
        kvp = kvc; kvc = nextslot(kvc); \
        const lds_cptr kp0 = lds + kvc + kb0, kp1 = lds + kvc + (kb0 ^ 32u); \
        float sacc = P[0] + P[1]; \
        GAPA(0, C, P); GAPA(1, C, P); GAPA(2, C, P); GAPA(3, C, P); GAPA(4, C, P); GAPA(5, C, P); GAPA(6, C, P); GAPA(7, C, P); \
        lsum += sacc; \
        PVM(0); PVM(1); SBAR(); \
        bool resc = false; float alpha = 1.f, lold = lsum; \
        if (MODE == 0) { \
          const float rm = rowmax16(C);                          \
          if (__builtin_expect(__any(rm > THR), 0)) { const float dl = fmaxf(rm, 0.f); alpha = __builtin_amdgcn_exp2f(-dl); lsum *= alpha; resc = true; \
            const float nm_ = negm[0] - dl; SETM(nm_); _Pragma("unroll") for (int r = 0; r < 16; ++r) C[r] -= dl; } \
        } else if (MODE == 1) { const float m0 = rowmax16(C); SETM(-m0); lsum = 0.f; _Pragma("unroll") for (int r = 0; r < 16; ++r) C[r] -= m0; } \
        SBAR(); \
        if (MODE != 2) { \
          KRD(0); KRD(1); QRD(0); PVM(2); C[0] = EX(C[0]); C[1] = EX(C[1]); C[2] = EX(C[2]); PIN(C); SBAR(); \
          KRD(2); QRD(1); QRD(2); PVM(3); C[3] = EX(C[3]); C[4] = EX(C[4]); C[5] = EX(C[5]); PIN(C); SBAR(); \
          KRD(3); KRD(4); QRD(3); PVM(4); C[6] = EX(C[6]); C[7] = EX(C[7]); C[8] = EX(C[8]); PIN(C); SBAR(); \
          KRD(5); QRD(4); QRD(5); PVM(5); C[9] = EX(C[9]); C[10] = EX(C[10]); C[11] = EX(C[11]); PIN(C); SBAR(); \
          KRD(6); QRD(6); PVM(6); C[12] = EX(C[12]); C[13] = EX(C[13]); PIN(C); SBAR(); \
          KRD(7); QRD(7); PVM(7); C[14] = EX(C[14]); C[15] = EX(C[15]); PIN(C); SBAR(); \
        } else { PVM(2); PVM(3); PVM(4); PVM(5); PVM(6); PVM(7); } \
        if (MODE == 0) { if (resc) { _Pragma("unroll") for (int dt = 0; dt < 4; ++dt) _Pragma("unroll") for (int r = 0; r < 16; ++r) o[dt][r] *= alpha; } } \
        else {   \
          auto rr = __builtin_amdgcn_permlane32_swap(__float_as_uint(lold), __float_as_uint(lold), false, false); \
          const float inv = 1.0f / (__uint_as_float(rr[0]) + __uint_as_float(rr[1])); \
          _Pragma("unroll") for (int dt = 0; dt < 4; ++dt) { st128(qo + (2 * dt) * 1024, pack8s(o[dt], 0, inv)); st128(qo + (2 * dt + 1) * 1024, pack8s(o[dt], 1, inv)); } \
          if (MODE == 1) { SBAR(); KRD_(0); KRD_(1); KRD_(2); KRD_(3); KRD_(4); KRD_(5); KRD_(6); KRD_(7); } \
        } \
        BAR(); } while (0)
      lds_cptr qsrc = lds + Q1_OFF + qb * 8192 + lane * 16;
#pragma unroll 1
      for (int u = 0; u < 4; ++u) {
        STEP(pB, pA, 0, true, false);
#pragma unroll 1
        for (int t = 0; t < 6; ++t) { STEP(pA, pB, 0, false, false); STEP(pB, pA, 0, false, false); }
        STEP(pA, pB, 0, false, false);
        STEP(pB, pA, 0, false, true);
        qsrc = qo;
        if (u == 3) break;
        STEP(pA, pB, 1, false, false);
        if (w == 0) STAMP(2 + u);
      }
      STEP(pA, pB, 2, false, false);
      if (w == 0) STAMP(5);
#undef STEP
#undef PVM
#undef QRD
#undef GAPA
#undef VRD
#undef KRD
#undef KRD_
#undef EX
#undef SETM
      BAR();
    }
  } else if (w == 3) {
    bf16x8 q2[2][3][8];
    {
      f32x16 acc[4][3];
      qphase(acc, 2);
#pragma unroll
      for (int q = 0; q < 3; ++q)
#pragma unroll
        for (int d = 0; d < 4; ++d) { q2[0][q][2 * d] = asfrag(pack8s(acc[d][q], 0, C_SCALE)); q2[0][q][2 * d + 1] = asfrag(pack8s(acc[d][q], 1, C_SCALE)); }
      BAR();
    }
    {
      LANE_CONSTS();
      const u16* kg = br ? kB : kA; const u16* vg = br ? vB : vA; const u16* wog = br ? woB : woA;
      const size_t kvbase = ((size_t)b * 32 + 4 * g) * (512 * 128);
      const __amdgpu_buffer_rsrc_t srdK = __builtin_amdgcn_make_buffer_rsrc((void*)(kg + kvbase), 0, 4 * 512 * 128 * 2, 0x00020000);
      const __amdgpu_buffer_rsrc_t srdV = __builtin_amdgcn_make_buffer_rsrc((void*)(vg + kvbase), 0, 4 * 512 * 128 * 2, 0x00020000);
      const __amdgpu_buffer_rsrc_t srdWo = __builtin_amdgcn_make_buffer_rsrc((void*)(wog + (size_t)g * 512 * 512), 0, 512 * 512 * 2, 0x00020000);
      const unsigned voffV = r8 * 256u + 64u * h + 16u * slot4;
      const unsigned voffK0 = r8 * 256u + 64u * h + 16u * (slot4 ^ (r8 >> 2)), voffK1 = voffK0 ^ 32u;
      const unsigned voffWo = r8 * 1024u + 64u * h + 16u * slot4;
      auto issue_kv = [&](int T, int slot) {
        const unsigned so = (((unsigned)T & ~15u) | (((unsigned)T + krot) & 15u)) * 8192u, dst = lds0 + slot;
#pragma unroll
        for (int i = 0; i < 8; ++i) dma16nt(srdK, ((i >> 1) & 1) ? voffK1 : voffK0, so + 2048u * (i >> 1) + 128u * (i & 1), dst + 1024u * i);
#pragma unroll
        for (int i = 0; i < 8; ++i) dma16nt(srdV, voffV, so + 2048u * (i >> 1) + 128u * (i & 1), dst + 8192u + 1024u * i);
      };
      auto issue_woc = [&](int r, int a, int dstoff) {
        const unsigned rr = (r + (b >> 1)) & 7;
        const unsigned so = (384u + 16u * rr) * 1024u + ((a ^ b) & 1) * 512u, dst = lds0 + dstoff;
#pragma unroll
        for (int i = 0; i < 8; ++i) dma16(srdWo, voffWo, so + 8192u * (i >> 2) + 128u * (i & 3), dst + 1024u * i);
      };
      {
        const lds_cptr h0 = lds + WO_OFF + lane * 16, h2 = lds + R3B_OFF + lane * 16;
#pragma unroll
        for (int s = 0; s < 8; ++s) { q2[1][0][s] = ld128(h0 + s * 1024); q2[1][1][s] = ld128(h0 + 8192 + s * 1024); q2[1][2][s] = ld128(h2 + s * 1024); }
      }
      BAR();
      BAR();
      int islot = KV_FIRST;
      const lds_ptr qo = lds + QO_OFF + lane * 16;
      for (int T = 2; T < 60; ++T) {
        const unsigned long long tmin = __builtin_amdgcn_s_memrealtime() + PACE;
        issue_kv(T + 4, islot);
        islot = (islot == KV_END - KV_SLOT) ? KV_OFF : islot + KV_SLOT;
        if (T == 30) {
#pragma unroll
          for (int q = 0; q < 3; ++q)
#pragma unroll
            for (int s = 0; s < 8; ++s) st128(qo + q * 8192 + s * 1024, __builtin_bit_cast(u32x4, q2[0][q][s]));
        }
        if (T == 46) {
#pragma unroll
          for (int q = 0; q < 3; ++q)
#pragma unroll
            for (int s = 0; s < 8; ++s) st128(qo + q * 8192 + s * 1024, __builtin_bit_cast(u32x4, q2[1][q][s]));
        }
        WAITVM(32);
        while (__builtin_amdgcn_s_memrealtime() < tmin) __builtin_amdgcn_s_sleep(1);
        BAR();
      }
      issue_woc(0, 0, TAILC(0)); issue_woc(1, 0, TAILC(1)); issue_woc(2, 0, TAILC(2)); issue_woc(3, 0, TAILC(3)); WAITVM(48); BAR();
      issue_woc(4, 0, TAILC(4)); issue_woc(5, 0, TAILC(5)); WAITVM(48); BAR();
      issue_woc(6, 0, TAILC(6)); issue_woc(7, 0, TAILC(7)); BAR();
      issue_woc(0, 1, TAILC(8)); issue_woc(1, 1, TAILC(9)); BAR();
      issue_woc(5, 1, TAILC(13)); WAITVM(0); BAR();
      issue_woc(7, 1, TAILC(15));
      if (lane == 0) *(LAS unsigned*)(lds + RDY_OFF) = 0u;
      WAITVM(0);
      BAR();
    }
  } else {
    const int pw = w - 4;
    const u16* wog = br ? woB : woA;
    {
      LANE_CONSTS();
      const u16* xg = br ? xB : xA; const u16* wqg = br ? wqB : wqA; const u16* kg = br ? kB : kA; const u16* vg = br ? vB : vA;
      const size_t kvbase = ((size_t)b * 32 + 4 * g) * (512 * 128);
      const __amdgpu_buffer_rsrc_t srdWq = __builtin_amdgcn_make_buffer_rsrc((void*)(wqg + g * 512), 0, 512 * 4096 * 2 - g * 1024, 0x00020000);
      const __amdgpu_buffer_rsrc_t srdX = __builtin_amdgcn_make_buffer_rsrc((void*)(xg + (size_t)b * 96 * 512), 0, 96 * 512 * 2, 0x00020000);
      const __amdgpu_buffer_rsrc_t srdK = __builtin_amdgcn_make_buffer_rsrc((void*)(kg + kvbase), 0, 4 * 512 * 128 * 2, 0x00020000);
      const __amdgpu_buffer_rsrc_t srdV = __builtin_amdgcn_make_buffer_rsrc((void*)(vg + kvbase), 0, 4 * 512 * 128 * 2, 0x00020000);
      const unsigned voffWq = r8 * 8192u + 64u * h + 16u * slot4;
      const unsigned voffX0 = r8 * 1024u + 64u * h + 16u * (slot4 ^ (r8 >> 2)), voffX1 = voffX0 ^ 32u;
      const unsigned voffK = (8u * pw + r8) * 256u + 64u * h + 16u * (slot4 ^ (r8 >> 2) ^ (2u * (pw & 1)));
      const unsigned voffV = (8u * pw + r8) * 256u + 64u * h + 16u * slot4;
      const unsigned rot = 2u * (b & 7);
      auto issue_w = [&](int j, int st) {
        const unsigned jj = (j + rot) & 15;
        const unsigned so = jj * (32u * 8192u) + pw * 256u, dst = lds0 + st + 8192u * pw;
#pragma unroll
        for (int i = 0; i < 8; ++i) dma16nt(srdWq, voffWq, so + 65536u * (i >> 1) + 128u * (i & 1), dst + 1024u * i);
      };
      auto issue_x = [&](int j) {
        const unsigned jj = (j + rot) & 15;
        const unsigned dst = lds0 + ((j & 2) ? XS_1 : XS_0) + 3072u * pw;
#pragma unroll
        for (int i = 0; i < 3; ++i) dma16nt(srdX, ((3 * pw + i) & 1) ? voffX1 : voffX0, jj * 64u + (3u * pw + i) * 8192u, dst + 1024u * i);
      };
      auto issue_t = [&](int T) {
        const unsigned so = (((unsigned)T + krot) & 15u) * 8192u, dst = lds0 + KV_OFF + ((T + 4) % 6) * KV_SLOT + 2048u * pw;
        dma16nt(srdK, voffK, so, dst); dma16nt(srdK, voffK, so + 128u, dst + 1024u);
        dma16nt(srdV, voffV, so, dst + 8192u); dma16nt(srdV, voffV, so + 128u, dst + 8192u + 1024u);
      };
      auto nextst = [](int st) { return (st == WS_A) ? WS_C : (st == WS_C) ? WS_B : WS_A; };
      issue_x(0);
      issue_w(0, WS_A);
      issue_w(1, WS_C);
      WAITVM(8);
      BAR();
      int st2 = WS_B;
      issue_x(2); issue_w(2, st2); st2 = nextst(st2); WAITVM(11); BAR();
      for (int j = 1; j < 13; j += 2) {
        if (j == 5) issue_t(0);
        if (j == 9) issue_t(1);
        issue_w(j + 2, st2); st2 = nextst(st2); WAITVM(8); BAR();
        issue_x(j + 3); issue_w(j + 3, st2); st2 = nextst(st2); WAITVM(11); BAR();
      }
      issue_w(15, st2); WAITVM(8); BAR();
      issue_t(2); WAITVM(4); BAR();
      issue_t(3); issue_t(4); WAITVM(12); BAR();
      BAR();
      WAITVM(8); BAR();
      issue_t(5); WAITVM(8); BAR();
      WAITVM(4); BAR();
      WAITVM(0); BAR();
      if (pw == 3) { const unsigned long long g0 = __hip_atomic_load(flagp, __ATOMIC_RELAXED, __HIP_MEMORY_SCOPE_AGENT);
        gen_lo = __builtin_amdgcn_readfirstlane((unsigned)g0); gen_hi = __builtin_amdgcn_readfirstlane((unsigned)(g0 >> 32)); }
      for (int T = 4; T < 15; ++T) BAR();
    }
    LANE_CONSTS();
    const __amdgpu_buffer_rsrc_t srdWo = __builtin_amdgcn_make_buffer_rsrc((void*)(wog + (size_t)g * 512 * 512), 0, 512 * 512 * 2, 0x00020000);
    const unsigned voffWo = (8u * (pw >> 1) + r8) * 1024u + 64u * (4u * (pw & 1) + h) + 16u * slot4;
    auto issue_wo = [&](int u, int m, int slot) {
      const unsigned rr = ((m >> 1) + (b >> 1)) & 7;
      const unsigned so = (128u * u + 16u * rr) * 1024u + ((m ^ b) & 1) * 512u;
      const unsigned dst = lds0 + WO_OFF + slot + 2048u * pw;
      dma16(srdWo, voffWo, so, dst);
      dma16(srdWo, voffWo, so + 128u, dst + 1024u);
    };
    const unsigned ob0 = 256u * h + 64u * q4 + 32u * blk + 16u * (p & 1) + 8u * (p >> 1) + 1024u * pw;
    const lds_cptr qo = lds + QO_OFF + lane * 16;
    f32x16 acc[2][2][3];
#pragma unroll
    for (int a = 0; a < 2; ++a)
#pragma unroll
      for (int c = 0; c < 2; ++c)
#pragma unroll
        for (int qb = 0; qb < 3; ++qb) acc[a][c][qb] = f32x16{};
    bf16x8 of[3], ofn[3];
    auto issue_M = [&](int M, int slot) { if (M < 48) issue_wo(M >> 4, M & 15, slot); else issue_wo(3, (M == 48) ? 5 : (M == 49) ? 7 : (M == 50) ? 9 : 13, slot); };
    issue_wo(0, 0, 0 * WO_SLOT);  BAR();
    issue_wo(0, 1, 1 * WO_SLOT);  WAITVM(2); BAR();
    issue_wo(0, 2, 2 * WO_SLOT); issue_wo(0, 3, 3 * WO_SLOT);
    int cur = 0, nxt = 4 * WO_SLOT;
    auto rot = [&]() { cur = (cur == 4 * WO_SLOT) ? 0 : cur + WO_SLOT; nxt = (nxt == 4 * WO_SLOT) ? 0 : nxt + WO_SLOT; };
    const int rb = b >> 1;
    for (int M = 0; M < 48; M += 2) {
      const int m = M & 15;
      {
        issue_M(M + 4, nxt);
        if (m == 0) {
#pragma unroll
          for (int qb = 0; qb < 3; ++qb) of[qb] = ld128(qo + qb * 8192 + (rb & 7) * 1024);
        }
        if (m != 14) {
#pragma unroll
          for (int qb = 0; qb < 3; ++qb) ofn[qb] = ld128(qo + qb * 8192 + (((m >> 1) + 1 + rb) & 7) * 1024);
        }
        const lds_cptr wp = lds + WO_OFF + cur + ob0;
#pragma unroll
        for (int c = 0; c < 2; ++c) {
          bf16x8 wf = ldtr2(wp + 512 * c, 4096);
#pragma unroll
          for (int qb = 0; qb < PMN; ++qb) acc[0][c][qb] = MFMA(wf, of[qb], acc[0][c][qb]);
        }
        WAITVM(6);
        BAR();
        rot();
      }
      {
        issue_M(M + 5, nxt);
        const lds_cptr wp = lds + WO_OFF + cur + ob0;
#pragma unroll
        for (int c = 0; c < 2; ++c) {
          bf16x8 wf = ldtr2(wp + 512 * c, 4096);
#pragma unroll
          for (int qb = 0; qb < PMN; ++qb) acc[1][c][qb] = MFMA(wf, of[qb], acc[1][c][qb]);
        }
#pragma unroll
        for (int qb = 0; qb < 3; ++qb) of[qb] = ofn[qb];
        if (M < 46) WAITVM(6); else WAITVM(0);
        BAR();
        rot();
      }
    }
    if (pw == 0) STAMP(7);
    const __amdgpu_buffer_rsrc_t srdS = __builtin_amdgcn_make_buffer_rsrc((void*)(ws + (size_t)((br * 16 + b) * 8 + g) * SLAB_BYTES), 0, (int)SLAB_BYTES, 0x00020000);
    {
      bf16x8 wf[2][2], ot[2][3];
      auto rd = [&](int i, int s) __attribute__((always_inline)) {
        const lds_cptr wp = lds + TAILC(i) + ob0;
        wf[s][0] = ldtr2(wp, 4096); wf[s][1] = ldtr2(wp + 512, 4096);
#pragma unroll
        for (int qb = 0; qb < 3; ++qb) ot[s][qb] = ld128(qo + qb * 8192 + (((i & 7) + rb) & 7) * 1024);
      };
      auto store_half = [&](int a) __attribute__((always_inline)) {
#pragma unroll
        for (int qb = 0; qb < 3; ++qb)
#pragma unroll
          for (int c = 0; c < 2; ++c)
#pragma unroll
            for (int s = 0; s < 2; ++s)
              __builtin_amdgcn_raw_buffer_store_b128(pack8n(acc[a][c][qb], s), srdS, lane * 16, (4 * (a ^ (b & 1)) + pw) * 12288 + ((qb * 2 + c) * 2 + s) * 1024, 16);
      };
      rd(0, 0);
#pragma unroll
      for (int i = 0; i < 16; ++i) {
        if (i == 14) BAR();
        if (i + 1 < 16) rd(i + 1, (i + 1) & 1);
        SBAR();
#pragma unroll
        for (int c = 0; c < 2; ++c)
#pragma unroll
          for (int qb = 0; qb < 3; ++qb) acc[i >> 3][c][qb] = MFMA(wf[i & 1][c], ot[i & 1][qb], acc[i >> 3][c][qb]);
        SBAR();
        if (i == 7) store_half(0);
      }
      store_half(1);
    }
    asm volatile("s_waitcnt vmcnt(0)" ::: "memory");
    if (pw == 0) STAMP(8);
  }
  asm volatile("s_waitcnt lgkmcnt(0)\n\ts_barrier" ::: "memory");
  LANE_CONSTS();
  volatile LAS unsigned* rdy = (volatile LAS unsigned*)(lds + RDY_OFF);
  if (w == 7) {
    const unsigned long long want = (((unsigned long long)gen_hi << 32) | gen_lo) + 1;
    if (lane == 0) __hip_atomic_store(flagp + 16 + g, want, __ATOMIC_RELAXED, __HIP_MEMORY_SCOPE_AGENT);
    unsigned m = 0;
    for (unsigned spins = 0; spins < (1u << 22); ++spins) {
      const unsigned long long f = __hip_atomic_load(flagp + 16 + (lane & 7), __ATOMIC_RELAXED, __HIP_MEMORY_SCOPE_AGENT);
      m = (unsigned)__ballot(f == want) & 0xffu;
      if (lane == 0) *rdy = m;
      if (m == 0xffu) break;
      __builtin_amdgcn_s_sleep(1);
    }
    if (lane == 0) *rdy = 0xffu;
    if (g == 0 && lane == 0) __hip_atomic_store(flagp, want, __ATOMIC_RELAXED, __HIP_MEMORY_SCOPE_AGENT);
    STAMP(9);
  } else {
    const __amdgpu_buffer_rsrc_t srdG = __builtin_amdgcn_make_buffer_rsrc((void*)(ws + (size_t)((br * 16 + b) * 8) * SLAB_BYTES), 0, (int)(8 * SLAB_BYTES), 0x00020000);
    const bool two = w < 5;
    u32x4 v[2][8];
    unsigned done = 0;
#pragma unroll 1
    for (int pass = 0; pass < (1 << 24); ++pass) {
      const unsigned m = __builtin_amdgcn_readfirstlane(*rdy);
#pragma unroll
      for (int i = 0; i < 8; ++i) {
        const int s = (g + i) & 7;
        if (((m >> s) & 1u) && !((done >> i) & 1u)) {
          const int so = s * (int)SLAB_BYTES + g * 12288 + w * 1024;
          v[0][i] = __builtin_amdgcn_raw_buffer_load_b128(srdG, lane * 16, so, 16);
          if (two) v[1][i] = __builtin_amdgcn_raw_buffer_load_b128(srdG, lane * 16, so + 7 * 1024, 16);
          done |= 1u << i;
        }
      }
      if (done == 0xffu) break;
      __builtin_amdgcn_s_sleep(1);
    }
#pragma unroll
    for (int i = 0; i < 2; ++i)
      if (i == 0 || two) {
        float a[8] = {0.f, 0.f, 0.f, 0.f, 0.f, 0.f, 0.f, 0.f};
#pragma unroll
        for (int s = 0; s < 8; ++s)
#pragma unroll
          for (int j = 0; j < 4; ++j) { a[2 * j] += __uint_as_float(v[i][s][j] << 16); a[2 * j + 1] += __uint_as_float(v[i][s][j] & 0xffff0000u); }
        const u32x4 r = {cvtpk(a[0], a[1]), cvtpk(a[2], a[3]), cvtpk(a[4], a[5]), cvtpk(a[6], a[7])};
        const int f = w + 7 * i, qb = f >> 2, c = (f >> 1) & 1, s = f & 1;
        const int q = 32 * qb + l32, n = 64 * g + 32 * c + 16 * s + 8 * h;
        *(u32x4*)(out + ((size_t)(b * 96 + q)) * 1024 + br * 512 + n) = r;
      }
  }
}

extern "C" void kernel_launch(void* const* d_in, const int* in_sizes, int n_in, void* d_out, int out_size, void* d_ws, size_t ws_size, hipStream_t stream) {
  (void)in_sizes; (void)n_in; (void)out_size; (void)ws_size;
  (void)hipFuncSetAttribute((const void*)fused_attn, hipFuncAttributeMaxDynamicSharedMemorySize, LDS_BYTES);
  hipLaunchKernelGGL(fused_attn, dim3(256), dim3(512), LDS_BYTES, stream,
                     (const u16*)d_in[0], (const u16*)d_in[1], (const u16*)d_in[2], (const u16*)d_in[3], (const u16*)d_in[4],
                     (const u16*)d_in[5], (const u16*)d_in[6], (const u16*)d_in[7], (const u16*)d_in[8], (const u16*)d_in[9], (char*)d_ws, (u16*)d_out
  );
}
```

```cpp
#include <hip/hip_runtime.h>
#include <stdint.h>

typedef unsigned short u16;
typedef short bf16x8 __attribute__((ext_vector_type(8)));
typedef short s16x4 __attribute__((ext_vector_type(4)));
typedef float f32x16 __attribute__((ext_vector_type(16)));
typedef float f32x2 __attribute__((ext_vector_type(2)));
typedef __bf16 bf16x2 __attribute__((ext_vector_type(2)));
typedef unsigned u32x4 __attribute__((ext_vector_type(4)));
#define LAS __attribute__((address_space(3)))
#define DI __device__ __forceinline__
typedef LAS const char* lds_cptr;
typedef LAS char* lds_ptr;
typedef __attribute__((address_space(1))) unsigned long long gu64;

constexpr int QO_OFF = 0;
constexpr int WO_OFF = 24576, WO_SLOT = 8192, WO_END = 65536;
constexpr int Q1_OFF = 40960;
constexpr int KV_OFF = 65536, KV_SLOT = 16384, KV_END = 163840;
constexpr int KV_FIRST = KV_OFF + 4 * KV_SLOT;
constexpr int WS_A = 0, WS_C = 49152, WS_B = 81920;
constexpr int XS_0 = 32768, XS_1 = 114688;
constexpr int kvhalf(int tile, int v) { return KV_OFF + ((tile + 4) % 6) * KV_SLOT + 8192 * v; }
constexpr int TAILC(int i) {
  return i == 0 ? kvhalf(58, 0) : i == 1 ? kvhalf(58, 1) : i == 2 ? kvhalf(59, 0) : i == 3 ? kvhalf(60, 0) : i == 4 ? kvhalf(59, 1) : i == 5 ? kvhalf(61, 0)
       : i == 6 ? kvhalf(60, 1) : i == 7 ? kvhalf(62, 0) : i == 8 ? kvhalf(61, 1) : i == 9 ? kvhalf(63, 0)
       : i == 10 ? WO_OFF + 3 * WO_SLOT : i == 11 ? WO_OFF + 4 * WO_SLOT : i == 12 ? WO_OFF + 0 * WO_SLOT : i == 13 ? kvhalf(62, 1) : i == 14 ? WO_OFF + 1 * WO_SLOT : kvhalf(63, 1);
}
constexpr int RDY_OFF = WO_OFF + 2 * WO_SLOT;
constexpr int OWN_OFF = KV_OFF;
constexpr int R3B_OFF = 114688;
constexpr int LDS_BYTES = 163840;
constexpr size_t SLAB_BYTES = 98304;
constexpr size_t FLAG_OFF = 32u << 20;
constexpr float C_SCALE = 0.08838834764831845f * 1.4426950408889634f;
constexpr float THR = 6.0f;

#define PMN 3
#define WO_DFLT
#define PACE2 48
#define PLO 15
#define PHI 28
#define WQN 8
#define ST_AUX 16
#define LD_AUX 16
#define PACE 30
#define NOPN "3"
#define SBAR() __builtin_amdgcn_sched_barrier(0)
#define PIN(x) asm volatile("" : "+v"(x))
#define MFMA(a, b, c) __builtin_amdgcn_mfma_f32_32x32x16_bf16((a), (b), (c), 0, 0, 0)

DI unsigned cvtpk(float lo, float hi) { f32x2 v = {lo, hi}; bf16x2 b = __builtin_convertvector(v, bf16x2); return __builtin_bit_cast(unsigned, b); }
DI bf16x8 ld128(lds_cptr p) { return *(const LAS bf16x8*)p; }
DI void st128(lds_ptr p, u32x4 v) { *(LAS u32x4*)p = v; }
DI s16x4 ldtr(lds_cptr p) { typedef short v4i16_t __attribute__((ext_vector_type(4))); return __builtin_bit_cast(s16x4, __builtin_amdgcn_ds_read_tr16_b64_v4i16((LAS v4i16_t*)p)); }
DI bf16x8 cat(s16x4 lo, s16x4 hi) { return __builtin_shufflevector(lo, hi, 0, 1, 2, 3, 4, 5, 6, 7); }
DI bf16x8 ldtr2(lds_cptr p, int second) { return cat(ldtr(p), ldtr(p + second)); }
DI u32x4 pack8s(const f32x16& x, int s, float sc) {
  u32x4 w = {cvtpk(x[8 * s] * sc, x[8 * s + 1] * sc), cvtpk(x[8 * s + 2] * sc, x[8 * s + 3] * sc), cvtpk(x[8 * s + 4] * sc, x[8 * s + 5] * sc), cvtpk(x[8 * s + 6] * sc, x[8 * s + 7] * sc)};
  return w;
}
DI u32x4 pack8n(const f32x16& x, int s) {
  u32x4 w = {cvtpk(x[8 * s], x[8 * s + 1]), cvtpk(x[8 * s + 2], x[8 * s + 3]), cvtpk(x[8 * s + 4], x[8 * s + 5]), cvtpk(x[8 * s + 6], x[8 * s + 7])};
  return w;
}
DI bf16x8 asfrag(u32x4 v) { return __builtin_bit_cast(bf16x8, v); }
DI void dma16(__amdgpu_buffer_rsrc_t srd, unsigned voff, unsigned soff, unsigned ldsaddr) {
  asm volatile("s_mov_b32 m0, %0\n\ts_nop " NOPN "\n\tbuffer_load_dwordx4 %1, %2, %3 offen lds" ::"s"(ldsaddr), "v"(voff), "s"(srd), "s"(soff) : "memory");
}
DI void dma16nt(__amdgpu_buffer_rsrc_t srd, unsigned voff, unsigned soff, unsigned ldsaddr) {
  asm volatile("s_mov_b32 m0, %0\n\ts_nop " NOPN "\n\tbuffer_load_dwordx4 %1, %2, %3 offen nt lds" ::"s"(ldsaddr), "v"(voff), "s"(srd), "s"(soff) : "memory");
}
DI int fresh_lane() { int l; asm volatile("v_mbcnt_lo_u32_b32 %0, -1, 0\n\tv_mbcnt_hi_u32_b32 %0, -1, %0" : "=v"(l)); return l; }
#define LANE_CONSTS() const int lane = fresh_lane(); const int l32 = lane & 31, h = lane >> 5; const int blk = (lane >> 4) & 1, q4 = (lane & 15) >> 2, p = lane & 3; \
  const unsigned r8 = (lane >> 2) & 7, slot4 = lane & 3; (void)l32; (void)h; (void)blk; (void)q4; (void)p; (void)r8; (void)slot4
#define WAITVM(n) asm volatile("s_waitcnt vmcnt(" #n ")" ::: "memory")

#define STAMP(i)
#define BAR() do { __builtin_amdgcn_sched_barrier(0); __builtin_amdgcn_s_waitcnt(0xC07F); asm volatile("s_barrier" ::: "memory"); } while (0)

DI float rowmax16(const f32x16& c) {
  float a = fmaxf(fmaxf(c[0], c[1]), c[2]), b = fmaxf(fmaxf(c[3], c[4]), c[5]);
  a = fmaxf(fmaxf(a, c[6]), c[7]); b = fmaxf(fmaxf(b, c[8]), c[9]);
  a = fmaxf(fmaxf(a, c[10]), c[11]); b = fmaxf(fmaxf(b, c[12]), c[13]);
  a = fmaxf(fmaxf(a, c[14]), c[15]);
  const float m = fmaxf(a, b);
  auto rr = __builtin_amdgcn_permlane32_swap(__float_as_uint(m), __float_as_uint(m), false, false);
  return fmaxf(__uint_as_float(rr[0]), __uint_as_float(rr[1]));
}

__global__ __launch_bounds__(512, 2) void fused_attn(const u16* __restrict__ xA, const u16* __restrict__ kA, const u16* __restrict__ vA, const u16* __restrict__ wqA,
                                                   const u16* __restrict__ woA, const u16* __restrict__ xB, const u16* __restrict__ kB, const u16* __restrict__ vB,
                                                   const u16* __restrict__ wqB, const u16* __restrict__ woB, char* __restrict__ ws, u16* __restrict__ out
) {
  extern __shared__ __attribute__((aligned(16))) char smem[];
  const lds_ptr lds = (lds_ptr)smem;
  const unsigned lds0 = (unsigned)(uintptr_t)smem;
  const int w = __builtin_amdgcn_readfirstlane(threadIdx.x >> 6);
  const int bid = blockIdx.x, xcd = bid & 7, idx = bid >> 3;
  const int br = xcd >> 2, g = 2 * (xcd & 3) + (idx & 1), b = idx >> 1;
  const unsigned krot = (idx + 2 * xcd) & 15;
  gu64* const flagp = (gu64*)(ws + FLAG_OFF + (size_t)(br * 16 + b) * 256);
  unsigned gen_lo = 0, gen_hi = 0;

  auto qphase = [&](f32x16 (&acc)[4][3], int head) __attribute__((always_inline)) {
    LANE_CONSTS();
    const unsigned wqb = 8192u * head + 2048u * h + 64u * q4 + 32u * blk + 16u * (p & 1) + 8u * (p >> 1);
    const unsigned xb0 = 1024u * (l32 >> 3) + 64u * (l32 & 7) + 16u * (h ^ ((l32 >> 2) & 3));
#pragma unroll
    for (int d = 0; d < 4; ++d)
#pragma unroll
      for (int q = 0; q < 3; ++q) acc[d][q] = f32x16{};
    bf16x8 wa[2], wb[2], x0[3], x1[3];
    __builtin_amdgcn_s_setprio(3);
    if (w == 0) STAMP(13);
    BAR();
    if (w == 0) STAMP(12);
#define QM(W, X, D0, i) acc[D0 + (i) / 3][(i) % 3] = MFMA(W[(i) / 3], X[(i) % 3], acc[D0 + (i) / 3][(i) % 3])
#define QW(KS, D) ldtr2(wp + 4096 * (KS) + 512 * (D), 256)
#define QMM(W, X, D0) do { QM(W, X, D0, 0); QM(W, X, D0, 1); QM(W, X, D0, 2); QM(W, X, D0, 3); QM(W, X, D0, 4); QM(W, X, D0, 5); SBAR(); } while (0)
#define QSTEP(j, FIRST) do { \
      const lds_cptr wp = lds + st + wqb; \
      const int xs = (((j) & 2) ? XS_1 : XS_0) + 512 * ((j) & 1); \
      const lds_cptr xp0 = lds + xs + xb0, xp1 = lds + xs + (xb0 ^ 32u); \
      if (FIRST) { x0[0] = ld128(xp0); x0[1] = ld128(xp0 + 4096); x0[2] = ld128(xp0 + 8192); wa[0] = QW(0, 0); wa[1] = QW(0, 1); SBAR(); } \
      else { \
        QM(wb, x1, 2, 0); x0[0] = ld128(xp0); x0[1] = ld128(xp0 + 4096); SBAR(); \
        QM(wb, x1, 2, 1); x0[2] = ld128(xp0 + 8192); wa[0] = QW(0, 0); SBAR(); \
        QM(wb, x1, 2, 2); wa[1] = QW(0, 1); SBAR(); \
        QM(wb, x1, 2, 3); QM(wb, x1, 2, 4); QM(wb, x1, 2, 5); SBAR(); } \
      QM(wa, x0, 0, 0); wb[0] = QW(0, 2); SBAR(); \
      QM(wa, x0, 0, 1); wb[1] = QW(0, 3); SBAR(); \
      QM(wa, x0, 0, 2); x1[0] = ld128(xp1); x1[1] = ld128(xp1 + 4096); SBAR(); \
      QM(wa, x0, 0, 3); x1[2] = ld128(xp1 + 8192); SBAR(); \
      QM(wa, x0, 0, 4); QM(wa, x0, 0, 5); SBAR(); \
      QM(wb, x0, 2, 0); wa[0] = QW(1, 0); SBAR(); \
      QM(wb, x0, 2, 1); wa[1] = QW(1, 1); SBAR(); \
      QM(wb, x0, 2, 2); QM(wb, x0, 2, 3); QM(wb, x0, 2, 4); QM(wb, x0, 2, 5); SBAR(); \
      QM(wa, x1, 0, 0); wb[0] = QW(1, 2); SBAR(); \
      QM(wa, x1, 0, 1); wb[1] = QW(1, 3); SBAR(); \
      QM(wa, x1, 0, 2); QM(wa, x1, 0, 3); QM(wa, x1, 0, 4); QM(wa, x1, 0, 5); \
      st = (st == WS_A) ? WS_C : (st == WS_C) ? WS_B : WS_A; \
      BAR(); } while (0)
    int st = WS_A;
    QSTEP(0, true);
#pragma unroll 1
    for (int j = 1; j < 16; ++j) {
      QSTEP(j, false);
    }
#undef QSTEP
    QMM(wb, x1, 2);
#undef QMM
#undef QM
#undef QW
  };

  if (w < 3) {
    {
      { const int lane = fresh_lane(); (void)lane;
      if (w == 0) { STAMP(0); const unsigned long long g0 = __hip_atomic_load(flagp, __ATOMIC_RELAXED, __HIP_MEMORY_SCOPE_AGENT);
          gen_lo = __builtin_amdgcn_readfirstlane((unsigned)g0); gen_hi = __builtin_amdgcn_readfirstlane((unsigned)(g0 >> 32)); } }
      f32x16 acc[4][3];
      qphase(acc, (w < 2) ? w : 3);
      const int lane = fresh_lane();
      const int o0 = (w == 0) ? QO_OFF : (w == 1) ? Q1_OFF : WO_OFF, o2 = (w == 0) ? QO_OFF + 16384 : (w == 1) ? Q1_OFF + 16384 : R3B_OFF;
#pragma unroll
      for (int q = 0; q < 3; ++q) {
        const lds_ptr dst = lds + ((q < 2) ? o0 + q * 8192 : o2) + lane * 16;
#pragma unroll
        for (int d = 0; d < 4; ++d) { st128(dst + (2 * d) * 1024, pack8s(acc[d][q], 0, C_SCALE)); st128(dst + (2 * d + 1) * 1024, pack8s(acc[d][q], 1, C_SCALE)); }
      }
      BAR();
    }
    {
      const int qb = w;
      LANE_CONSTS();
      if (w == 0) STAMP(1);

      const unsigned kb0 = 2048u * (l32 >> 3) + 64u * (l32 & 7) + 16u * (h ^ ((l32 >> 2) & 3));
      const unsigned vb0 = 8192u + 256u * h + 64u * q4 + 32u * blk + 8u * p;
      const lds_ptr qo = lds + QO_OFF + qb * 8192 + lane * 16;
      f32x16 o[4], pA, pB, negm;
      bf16x8 qf[8], kf[8], vf[8];
      u32x4 pw0, pw1;
      float lsum = 0.f;
      int kvp = KV_FIRST, kvc = KV_FIRST;
      auto nextslot = [](int s) { return (s == KV_END - KV_SLOT) ? KV_OFF : s + KV_SLOT; };
#define EX(v) __builtin_amdgcn_exp2f(v)
#define SETM(x) do { _Pragma("unroll") for (int r_ = 0; r_ < 16; ++r_) negm[r_] = (x); PIN(negm); } while (0)
#define KRD_(i) kf[i] = ld128(((i & 1) ? kp1 : kp0) + 512 * (i >> 1))
#define KRD(i) do { if (MODE != 1) KRD_(i); } while (0)
#define VRD(i) vf[i] = ldtr2(vp + 4096 * ((i) >> 2) + 512 * ((i) & 3), 2048)
      {
#pragma unroll
        for (int s = 0; s < 8; ++s) qf[s] = ld128(qo + s * 1024);
        { const lds_cptr kp0 = lds + kvc + kb0, kp1 = lds + kvc + (kb0 ^ 32u);
#pragma unroll
          for (int s = 0; s < 8; ++s) KRD_(s); }
        pA = MFMA(kf[0], qf[0], f32x16{});
#pragma unroll
        for (int s = 1; s < 8; ++s) pA = MFMA(kf[s], qf[s], pA);
        SBAR();
        kvc = nextslot(kvc);
        { const lds_cptr kp0 = lds + kvc + kb0, kp1 = lds + kvc + (kb0 ^ 32u);
#pragma unroll
          for (int s = 0; s < 8; ++s) KRD_(s); }
        const float m0 = rowmax16(pA);
        SETM(-m0);
#pragma unroll
        for (int r = 0; r < 16; ++r) pA[r] = EX(pA[r] - m0);
        BAR();
      }
#define GAPA(i, C, P) do { VRD(i); if (MODE != 2) { if (i == 0) C = MFMA(kf[0], qf[0], MODE == 0 ? negm : f32x16{}); else C = MFMA(kf[i], qf[i], C); } \
        if (i < 7) { sacc += P[2 * i + 2]; sacc += P[2 * i + 3]; } \
        if (i < 4) pw0[i] = cvtpk(P[2 * i], P[2 * i + 1]); else pw1[i - 4] = cvtpk(P[2 * i], P[2 * i + 1]); \
        PIN(sacc); if (i < 4) PIN(pw0); else PIN(pw1); SBAR(); } while (0)
#define PVM(i) o[(i) & 3] = MFMA(vf[i], asfrag(((i) < 4) ? pw0 : pw1), (ZC && (i) < 4) ? f32x16{} : o[(i) & 3])
#define QRD(i) do { if (QPF) qf[i] = ld128(qsrc + (i) * 1024); } while (0)
#define STEP(C, P, MODE_, ZC_, QPF_) do { constexpr int MODE = MODE_; constexpr bool ZC = ZC_, QPF = QPF_; \
        const lds_cptr vp = lds + kvp + vb0; \
        kvp = kvc; kvc = nextslot(kvc); \
        const lds_cptr kp0 = lds + kvc + kb0, kp1 = lds + kvc + (kb0 ^ 32u); \
        float sacc = P[0] + P[1]; \
        GAPA(0, C, P); GAPA(1, C, P); GAPA(2, C, P); GAPA(3, C, P); GAPA(4, C, P); GAPA(5, C, P); GAPA(6, C, P); GAPA(7, C, P); \
        lsum += sacc; \
        PVM(0); PVM(1); SBAR(); \
        bool resc = false; float alpha = 1.f, lold = lsum; \
        if (MODE == 0) { \
          const float rm = rowmax16(C);                          \
          if (__builtin_expect(__any(rm > THR), 0)) { const float dl = fmaxf(rm, 0.f); alpha = __builtin_amdgcn_exp2f(-dl); lsum *= alpha; resc = true; \
            const float nm_ = negm[0] - dl; SETM(nm_); _Pragma("unroll") for (int r = 0; r < 16; ++r) C[r] -= dl; } \
        } else if (MODE == 1) { const float m0 = rowmax16(C); SETM(-m0); lsum = 0.f; _Pragma("unroll") for (int r = 0; r < 16; ++r) C[r] -= m0; } \
        SBAR(); \
        if (MODE != 2) { \
          KRD(0); KRD(1); QRD(0); PVM(2); C[0] = EX(C[0]); C[1] = EX(C[1]); C[2] = EX(C[2]); PIN(C); SBAR(); \
          KRD(2); QRD(1); QRD(2); PVM(3); C[3] = EX(C[3]); C[4] = EX(C[4]); C[5] = EX(C[5]); PIN(C); SBAR(); \
          KRD(3); KRD(4); QRD(3); PVM(4); C[6] = EX(C[6]); C[7] = EX(C[7]); C[8] = EX(C[8]); PIN(C); SBAR(); \
          KRD(5); QRD(4); QRD(5); PVM(5); C[9] = EX(C[9]); C[10] = EX(C[10]); C[11] = EX(C[11]); PIN(C); SBAR(); \
          KRD(6); QRD(6); PVM(6); C[12] = EX(C[12]); C[13] = EX(C[13]); PIN(C); SBAR(); \
          KRD(7); QRD(7); PVM(7); C[14] = EX(C[14]); C[15] = EX(C[15]); PIN(C); SBAR(); \
        } else { PVM(2); PVM(3); PVM(4); PVM(5); PVM(6); PVM(7); } \
        if (MODE == 0) { if (resc) { _Pragma("unroll") for (int dt = 0; dt < 4; ++dt) _Pragma("unroll") for (int r = 0; r < 16; ++r) o[dt][r] *= alpha; } } \
        else {   \
          auto rr = __builtin_amdgcn_permlane32_swap(__float_as_uint(lold), __float_as_uint(lold), false, false); \
          const float inv = 1.0f / (__uint_as_float(rr[0]) + __uint_as_float(rr[1])); \
          _Pragma("unroll") for (int dt = 0; dt < 4; ++dt) { st128(qo + (2 * dt) * 1024, pack8s(o[dt], 0, inv)); st128(qo + (2 * dt + 1) * 1024, pack8s(o[dt], 1, inv)); } \
          if (MODE == 1) { SBAR(); KRD_(0); KRD_(1); KRD_(2); KRD_(3); KRD_(4); KRD_(5); KRD_(6); KRD_(7); } \
        } \
        BAR(); } while (0)
      lds_cptr qsrc = lds + Q1_OFF + qb * 8192 + lane * 16;
#pragma unroll 1
      for (int u = 0; u < 4; ++u) {
        STEP(pB, pA, 0, true, false);
#pragma unroll 1
        for (int t = 0; t < 6; ++t) { STEP(pA, pB, 0, false, false); STEP(pB, pA, 0, false, false); }
        STEP(pA, pB, 0, false, false);
        STEP(pB, pA, 0, false, true);
        qsrc = qo;
        if (u == 3) break;
        STEP(pA, pB, 1, false, false);
        if (w == 0) STAMP(2 + u);
      }
      STEP(pA, pB, 2, false, false);
      if (w == 0) STAMP(5);
#undef STEP
#undef PVM
#undef QRD
#undef GAPA
#undef VRD
#undef KRD
#undef KRD_
#undef EX
#undef SETM
      BAR();
    }
  } else if (w == 3) {
    bf16x8 q2[2][3][8];
    {
      f32x16 acc[4][3];
      qphase(acc, 2);
#pragma unroll
      for (int q = 0; q < 3; ++q)
#pragma unroll
        for (int d = 0; d < 4; ++d) { q2[0][q][2 * d] = asfrag(pack8s(acc[d][q], 0, C_SCALE)); q2[0][q][2 * d + 1] = asfrag(pack8s(acc[d][q], 1, C_SCALE)); }
      BAR();
    }
    {
      LANE_CONSTS();
      const u16* kg = br ? kB : kA; const u16* vg = br ? vB : vA; const u16* wog = br ? woB : woA;
      const size_t kvbase = ((size_t)b * 32 + 4 * g) * (512 * 128);
      const __amdgpu_buffer_rsrc_t srdK = __builtin_amdgcn_make_buffer_rsrc((void*)(kg + kvbase), 0, 4 * 512 * 128 * 2, 0x00020000);
      const __amdgpu_buffer_rsrc_t srdV = __builtin_amdgcn_make_buffer_rsrc((void*)(vg + kvbase), 0, 4 * 512 * 128 * 2, 0x00020000);
      const __amdgpu_buffer_rsrc_t srdWo = __builtin_amdgcn_make_buffer_rsrc((void*)(wog + (size_t)g * 512 * 512), 0, 512 * 512 * 2, 0x00020000);
      const unsigned voffV = r8 * 256u + 64u * h + 16u * slot4;
      const unsigned voffK0 = r8 * 256u + 64u * h + 16u * (slot4 ^ (r8 >> 2)), voffK1 = voffK0 ^ 32u;
      const unsigned voffWo = r8 * 1024u + 64u * h + 16u * slot4;
      auto issue_kv = [&](int T, int slot) {
        const unsigned so = (((unsigned)T & ~15u) | (((unsigned)T + krot) & 15u)) * 8192u, dst = lds0 + slot;
#pragma unroll
        for (int i = 0; i < 8; ++i) dma16nt(srdK, ((i >> 1) & 1) ? voffK1 : voffK0, so + 2048u * (i >> 1) + 128u * (i & 1), dst + 1024u * i);
#pragma unroll
        for (int i = 0; i < 8; ++i) dma16nt(srdV, voffV, so + 2048u * (i >> 1) + 128u * (i & 1), dst + 8192u + 1024u * i);
      };
      auto issue_woc = [&](int r, int a, int dstoff) {
        const unsigned rr = (r + (b >> 1)) & 7;
        const unsigned so = (384u + 16u * rr) * 1024u + ((a ^ b) & 1) * 512u, dst = lds0 + dstoff;
#pragma unroll
        for (int i = 0; i < 8; ++i) dma16(srdWo, voffWo, so + 8192u * (i >> 2) + 128u * (i & 3), dst + 1024u * i);
      };
      {
        const lds_cptr h0 = lds + WO_OFF + lane * 16, h2 = lds + R3B_OFF + lane * 16;
#pragma unroll
        for (int s = 0; s < 8; ++s) { q2[1][0][s] = ld128(h0 + s * 1024); q2[1][1][s] = ld128(h0 + 8192 + s * 1024); q2[1][2][s] = ld128(h2 + s * 1024); }
      }
      BAR();
      BAR();
      int islot = KV_FIRST;
      const lds_ptr qo = lds + QO_OFF + lane * 16;
      for (int T = 2; T < 60; ++T) {
        const unsigned long long tmin = __builtin_amdgcn_s_memrealtime() + ((T >= PLO && T < PHI) ? PACE2 : PACE);
        issue_kv(T + 4, islot);
        islot = (islot == KV_END - KV_SLOT) ? KV_OFF : islot + KV_SLOT;
        if (T == 30) {
#pragma unroll
          for (int q = 0; q < 3; ++q)
#pragma unroll
            for (int s = 0; s < 8; ++s) st128(qo + q * 8192 + s * 1024, __builtin_bit_cast(u32x4, q2[0][q][s]));
        }
        if (T == 46) {
#pragma unroll
          for (int q = 0; q < 3; ++q)
#pragma unroll
            for (int s = 0; s < 8; ++s) st128(qo + q * 8192 + s * 1024, __builtin_bit_cast(u32x4, q2[1][q][s]));
        }
        WAITVM(32);
        while (__builtin_amdgcn_s_memrealtime() < tmin) __builtin_amdgcn_s_sleep(1);
        BAR();
      }
      issue_woc(0, 0, TAILC(0)); issue_woc(1, 0, TAILC(1)); issue_woc(2, 0, TAILC(2)); issue_woc(3, 0, TAILC(3)); WAITVM(48); BAR();
      issue_woc(4, 0, TAILC(4)); issue_woc(5, 0, TAILC(5)); WAITVM(48); BAR();
      issue_woc(6, 0, TAILC(6)); issue_woc(7, 0, TAILC(7)); BAR();
      issue_woc(0, 1, TAILC(8)); issue_woc(1, 1, TAILC(9)); BAR();
      issue_woc(5, 1, TAILC(13)); WAITVM(0); BAR();
      issue_woc(7, 1, TAILC(15));
      if (lane == 0) { *(LAS unsigned*)(lds + RDY_OFF) = 0u; *(LAS unsigned*)(lds + RDY_OFF + 4) = 0u; }
      WAITVM(0);
      BAR();
    }
  } else {
    const int pw = w - 4;
    const u16* wog = br ? woB : woA;
    {
      LANE_CONSTS();
      const u16* xg = br ? xB : xA; const u16* wqg = br ? wqB : wqA; const u16* kg = br ? kB : kA; const u16* vg = br ? vB : vA;
      const size_t kvbase = ((size_t)b * 32 + 4 * g) * (512 * 128);
      const __amdgpu_buffer_rsrc_t srdWq = __builtin_amdgcn_make_buffer_rsrc((void*)(wqg + g * 512), 0, 512 * 4096 * 2 - g * 1024, 0x00020000);
      const __amdgpu_buffer_rsrc_t srdX = __builtin_amdgcn_make_buffer_rsrc((void*)(xg + (size_t)b * 96 * 512), 0, 96 * 512 * 2, 0x00020000);
      const __amdgpu_buffer_rsrc_t srdK = __builtin_amdgcn_make_buffer_rsrc((void*)(kg + kvbase), 0, 4 * 512 * 128 * 2, 0x00020000);
      const __amdgpu_buffer_rsrc_t srdV = __builtin_amdgcn_make_buffer_rsrc((void*)(vg + kvbase), 0, 4 * 512 * 128 * 2, 0x00020000);
      const unsigned voffWq = r8 * 8192u + 64u * h + 16u * slot4;
      const unsigned voffX0 = r8 * 1024u + 64u * h + 16u * (slot4 ^ (r8 >> 2)), voffX1 = voffX0 ^ 32u;
      const unsigned voffK = (8u * pw + r8) * 256u + 64u * h + 16u * (slot4 ^ (r8 >> 2) ^ (2u * (pw & 1)));
      const unsigned voffV = (8u * pw + r8) * 256u + 64u * h + 16u * slot4;
      const unsigned rot = 2u * (b & 7);
      auto issue_w = [&](int j, int st) {
        const unsigned jj = (j + rot) & 15;
        const unsigned so = jj * (32u * 8192u) + pw * 256u, dst = lds0 + st + 8192u * pw;
#pragma unroll
        for (int i = 0; i < WQN; ++i) dma16nt(srdWq, voffWq, so + 65536u * (i >> 1) + 128u * (i & 1), dst + 1024u * i);
      };
      auto issue_x = [&](int j) {
        const unsigned jj = (j + rot) & 15;
        const unsigned dst = lds0 + ((j & 2) ? XS_1 : XS_0) + 3072u * pw;
#pragma unroll
        for (int i = 0; i < 3; ++i) dma16nt(srdX, ((3 * pw + i) & 1) ? voffX1 : voffX0, jj * 64u + (3u * pw + i) * 8192u, dst + 1024u * i);
      };
      auto issue_t = [&](int T) {
        const unsigned so = (((unsigned)T + krot) & 15u) * 8192u, dst = lds0 + KV_OFF + ((T + 4) % 6) * KV_SLOT + 2048u * pw;
        dma16nt(srdK, voffK, so, dst); dma16nt(srdK, voffK, so + 128u, dst + 1024u);
        dma16nt(srdV, voffV, so, dst + 8192u); dma16nt(srdV, voffV, so + 128u, dst + 8192u + 1024u);
      };
      auto nextst = [](int st) { return (st == WS_A) ? WS_C : (st == WS_C) ? WS_B : WS_A; };
#define BARN() asm volatile("s_barrier" ::: "memory")
      issue_x(0);
      issue_w(0, WS_A);
      issue_w(1, WS_C);
      WAITVM(8);
      BARN();
      int st2 = WS_B;
#define QT(j, k)
#define LEVEN(j) do { issue_x((j) + 2); issue_w((j) + 2, st2); st2 = nextst(st2); QT(j, 0); WAITVM(11); QT(j, 1); BARN(); QT(j, 2); } while (0)
#define LODD(j) do { issue_w((j) + 2, st2); st2 = nextst(st2); QT(j, 0); WAITVM(8); QT(j, 1); BARN(); QT(j, 2); } while (0)
#define LODDT(j, T) do { issue_w((j) + 2, st2); st2 = nextst(st2); issue_t(T); QT(j, 0); WAITVM(12); QT(j, 1); BARN(); QT(j, 2); } while (0)
      LEVEN(0); LODD(1); LEVEN(2); LODD(3); LEVEN(4); LODDT(5, 0); LEVEN(6); LODD(7); LEVEN(8); LODDT(9, 1); LEVEN(10); LODD(11); LEVEN(12);
      issue_w(15, st2); WAITVM(8); BARN();
#undef LEVEN
#undef LODD
#undef LODDT
      issue_t(2); WAITVM(4); BAR();
      issue_t(3); issue_t(4); WAITVM(12); BAR();
      BAR();
      WAITVM(8); BAR();
      issue_t(5); WAITVM(8); BAR();
      WAITVM(4); BAR();
      WAITVM(0); BAR();
      if (pw == 3) { const unsigned long long g0 = __hip_atomic_load(flagp, __ATOMIC_RELAXED, __HIP_MEMORY_SCOPE_AGENT);
        gen_lo = __builtin_amdgcn_readfirstlane((unsigned)g0); gen_hi = __builtin_amdgcn_readfirstlane((unsigned)(g0 >> 32)); }
      for (int T = 4; T < 15; ++T) BAR();
    }
    LANE_CONSTS();
    const __amdgpu_buffer_rsrc_t srdWo = __builtin_amdgcn_make_buffer_rsrc((void*)(wog + (size_t)g * 512 * 512), 0, 512 * 512 * 2, 0x00020000);
    const unsigned voffWo = (8u * (pw >> 1) + r8) * 1024u + 64u * (4u * (pw & 1) + h) + 16u * slot4;
    auto issue_wo = [&](int u, int m, int slot) {
      const unsigned rr = ((m >> 1) + (b >> 1)) & 7;
      const unsigned so = (128u * u + 16u * rr) * 1024u + ((m ^ b) & 1) * 512u;
      const unsigned dst = lds0 + WO_OFF + slot + 2048u * pw;
      dma16(srdWo, voffWo, so, dst);
      dma16(srdWo, voffWo, so + 128u, dst + 1024u);
    };
    const unsigned ob0 = 256u * h + 64u * q4 + 32u * blk + 16u * (p & 1) + 8u * (p >> 1) + 1024u * pw;
    const lds_cptr qo = lds + QO_OFF + lane * 16;
    f32x16 acc[2][2][3];
#pragma unroll
    for (int a = 0; a < 2; ++a)
#pragma unroll
      for (int c = 0; c < 2; ++c)
#pragma unroll
        for (int qb = 0; qb < 3; ++qb) acc[a][c][qb] = f32x16{};
    bf16x8 of[3], ofn[3];
    auto issue_M = [&](int M, int slot) { if (M < 48) issue_wo(M >> 4, M & 15, slot); else issue_wo(3, (M == 48) ? 5 : (M == 49) ? 7 : (M == 50) ? 9 : 13, slot); };
    issue_wo(0, 0, 0 * WO_SLOT);  BAR();
    issue_wo(0, 1, 1 * WO_SLOT);  WAITVM(2); BAR();
    issue_wo(0, 2, 2 * WO_SLOT); issue_wo(0, 3, 3 * WO_SLOT);
    int cur = 0, nxt = 4 * WO_SLOT;
    auto rot = [&]() { cur = (cur == 4 * WO_SLOT) ? 0 : cur + WO_SLOT; nxt = (nxt == 4 * WO_SLOT) ? 0 : nxt + WO_SLOT; };
    const int rb = b >> 1;
    for (int M = 0; M < 48; M += 2) {
      const int m = M & 15;
      {
        issue_M(M + 4, nxt);
        if (m == 0) {
#pragma unroll
          for (int qb = 0; qb < 3; ++qb) of[qb] = ld128(qo + qb * 8192 + (rb & 7) * 1024);
        }
        if (m != 14) {
#pragma unroll
          for (int qb = 0; qb < 3; ++qb) ofn[qb] = ld128(qo + qb * 8192 + (((m >> 1) + 1 + rb) & 7) * 1024);
        }
        const lds_cptr wp = lds + WO_OFF + cur + ob0;
#pragma unroll
        for (int c = 0; c < 2; ++c) {
          bf16x8 wf = ldtr2(wp + 512 * c, 4096);
#pragma unroll
          for (int qb = 0; qb < PMN; ++qb) acc[0][c][qb] = MFMA(wf, of[qb], acc[0][c][qb]);
        }
        WAITVM(6);
        BAR();
        rot();
      }
      {
        issue_M(M + 5, nxt);
        const lds_cptr wp = lds + WO_OFF + cur + ob0;
#pragma unroll
        for (int c = 0; c < 2; ++c) {
          bf16x8 wf = ldtr2(wp + 512 * c, 4096);
#pragma unroll
          for (int qb = 0; qb < PMN; ++qb) acc[1][c][qb] = MFMA(wf, of[qb], acc[1][c][qb]);
        }
#pragma unroll
        for (int qb = 0; qb < 3; ++qb) of[qb] = ofn[qb];
        if (M < 46) WAITVM(6); else WAITVM(0);
        BAR();
        rot();
      }
    }
    if (pw == 0) STAMP(7);
    const __amdgpu_buffer_rsrc_t srdS = __builtin_amdgcn_make_buffer_rsrc((void*)(ws + (size_t)((br * 16 + b) * 8 + g) * SLAB_BYTES), 0, (int)SLAB_BYTES, 0x00020000);
    {
      bf16x8 wf[2][2], ot[2][3];
      auto rd = [&](int i, int s) __attribute__((always_inline)) {
        const lds_cptr wp = lds + TAILC(i) + ob0;
        wf[s][0] = ldtr2(wp, 4096); wf[s][1] = ldtr2(wp + 512, 4096);
#pragma unroll
        for (int qb = 0; qb < 3; ++qb) ot[s][qb] = ld128(qo + qb * 8192 + (((i & 7) + rb) & 7) * 1024);
      };
      auto store_half = [&](int a) __attribute__((always_inline)) {
#pragma unroll
        for (int qb = 0; qb < 3; ++qb)
#pragma unroll
          for (int c = 0; c < 2; ++c)
#pragma unroll
            for (int s = 0; s < 2; ++s)
              __builtin_amdgcn_raw_buffer_store_b128(pack8n(acc[a][c][qb], s), srdS, lane * 16, (4 * (a ^ (b & 1)) + pw) * 12288 + ((qb * 2 + c) * 2 + s) * 1024, ST_AUX);
      };
      const int own_a = (g >> 2) ^ (b & 1);
      rd(0, 0);
#pragma unroll
      for (int i = 0; i < 16; ++i) {
        if (i == 14) BAR();
        if (i + 1 < 16) rd(i + 1, (i + 1) & 1);
        SBAR();
#pragma unroll
        for (int c = 0; c < 2; ++c)
#pragma unroll
          for (int qb = 0; qb < 3; ++qb) acc[i >> 3][c][qb] = MFMA(wf[i & 1][c], ot[i & 1][qb], acc[i >> 3][c][qb]);
        SBAR();
        if (i == 7 && !(pw == (g & 3) && own_a == 0)) store_half(0);
      }
      if (!(pw == (g & 3) && own_a == 1)) store_half(1);
      asm volatile("s_waitcnt vmcnt(0)" ::: "memory");
      if (pw == 0) STAMP(8);
      asm volatile("s_waitcnt lgkmcnt(0)\n\ts_barrier" ::: "memory");
      if (pw == (g & 3)) {
        const lds_ptr op = lds + OWN_OFF + lane * 16;
#pragma unroll
        for (int qb = 0; qb < 3; ++qb)
#pragma unroll
          for (int c = 0; c < 2; ++c)
#pragma unroll
            for (int s = 0; s < 2; ++s) st128(op + ((qb * 2 + c) * 2 + s) * 1024, own_a ? pack8n(acc[1][c][qb], s) : pack8n(acc[0][c][qb], s));
        asm volatile("s_waitcnt lgkmcnt(0)" ::: "memory");
        if (lane == 0) *(volatile LAS unsigned*)(lds + RDY_OFF + 4) = 1u;
      }
    }
  }
  if (w < 4) asm volatile("s_waitcnt lgkmcnt(0)\n\ts_barrier" ::: "memory");
  LANE_CONSTS();
  volatile LAS unsigned* rdy = (volatile LAS unsigned*)(lds + RDY_OFF);
  if (w == 7) {
    const unsigned long long want = (((unsigned long long)gen_hi << 32) | gen_lo) + 1;
    if (lane == 0) __hip_atomic_store(flagp + 16 + g, want, __ATOMIC_RELAXED, __HIP_MEMORY_SCOPE_AGENT);
    unsigned m = 0;
    for (unsigned spins = 0; spins < (1u << 22); ++spins) {
      const unsigned long long f = __hip_atomic_load(flagp + 16 + (lane & 7), __ATOMIC_RELAXED, __HIP_MEMORY_SCOPE_AGENT);
      m = (unsigned)__ballot(f == want) & 0xffu;
      if (lane == 0) *rdy = m;
      if (m == 0xffu) break;
      __builtin_amdgcn_s_sleep(1);
    }
    if (lane == 0) *rdy = 0xffu;
    if (g == 0 && lane == 0) __hip_atomic_store(flagp, want, __ATOMIC_RELAXED, __HIP_MEMORY_SCOPE_AGENT);
    STAMP(9);
  } else {
    const __amdgpu_buffer_rsrc_t srdG = __builtin_amdgcn_make_buffer_rsrc((void*)(ws + (size_t)((br * 16 + b) * 8) * SLAB_BYTES), 0, (int)(8 * SLAB_BYTES), 0x00020000);
    const bool two = w < 5;
    u32x4 v[2][8];
    unsigned done = 1;
#pragma unroll 1
    for (int pass = 0; pass < (1 << 24); ++pass) {
      const unsigned m = __builtin_amdgcn_readfirstlane(*rdy);
#pragma unroll
      for (int i = 1; i < 8; ++i) {
        const int s = (g + i) & 7;
        if (((m >> s) & 1u) && !((done >> i) & 1u)) {
          const int so = s * (int)SLAB_BYTES + g * 12288 + w * 1024;
          v[0][i] = __builtin_amdgcn_raw_buffer_load_b128(srdG, lane * 16, so, LD_AUX);
          if (two) v[1][i] = __builtin_amdgcn_raw_buffer_load_b128(srdG, lane * 16, so + 7 * 1024, LD_AUX);
          done |= 1u << i;
        }
      }
      if (done == 0xffu) break;
      __builtin_amdgcn_s_sleep(1);
    }
    while (__builtin_amdgcn_readfirstlane(rdy[1]) == 0u) __builtin_amdgcn_s_sleep(1);
    v[0][0] = *(const LAS u32x4*)(lds + OWN_OFF + w * 1024 + lane * 16);
    if (two) v[1][0] = *(const LAS u32x4*)(lds + OWN_OFF + (w + 7) * 1024 + lane * 16);
#pragma unroll
    for (int i = 0; i < 2; ++i)
      if (i == 0 || two) {
        float a[8] = {0.f, 0.f, 0.f, 0.f, 0.f, 0.f, 0.f, 0.f};
#pragma unroll
        for (int s = 0; s < 8; ++s)
#pragma unroll
          for (int j = 0; j < 4; ++j) { a[2 * j] += __uint_as_float(v[i][s][j] << 16); a[2 * j + 1] += __uint_as_float(v[i][s][j] & 0xffff0000u); }
        const u32x4 r = {cvtpk(a[0], a[1]), cvtpk(a[2], a[3]), cvtpk(a[4], a[5]), cvtpk(a[6], a[7])};
        const int f = w + 7 * i, qb = f >> 2, c = (f >> 1) & 1, s = f & 1;
        const int q = 32 * qb + l32, n = 64 * g + 32 * c + 16 * s + 8 * h;
        *(u32x4*)(out + ((size_t)(b * 96 + q)) * 1024 + br * 512 + n) = r;
      }
  }
}

extern "C" void kernel_launch(void* const* d_in, const int* in_sizes, int n_in, void* d_out, int out_size, void* d_ws, size_t ws_size, hipStream_t stream) {
  (void)in_sizes; (void)n_in; (void)out_size; (void)ws_size;
  (void)hipFuncSetAttribute((const void*)fused_attn, hipFuncAttributeMaxDynamicSharedMemorySize, LDS_BYTES);
  hipLaunchKernelGGL(fused_attn, dim3(256), dim3(512), LDS_BYTES, stream,
                     (const u16*)d_in[0], (const u16*)d_in[1], (const u16*)d_in[2], (const u16*)d_in[3], (const u16*)d_in[4],
                     (const u16*)d_in[5], (const u16*)d_in[6], (const u16*)d_in[7], (const u16*)d_in[8], (const u16*)d_in[9], (char*)d_ws, (u16*)d_out
  );
}
```

```cpp
#include <hip/hip_runtime.h>
#include <stdint.h>

typedef unsigned short u16;
typedef short bf16x8 __attribute__((ext_vector_type(8)));
typedef short s16x4 __attribute__((ext_vector_type(4)));
typedef float f32x16 __attribute__((ext_vector_type(16)));
typedef float f32x2 __attribute__((ext_vector_type(2)));
typedef __bf16 bf16x2 __attribute__((ext_vector_type(2)));
typedef unsigned u32x4 __attribute__((ext_vector_type(4)));
#define LAS __attribute__((address_space(3)))
#define DI __device__ __forceinline__
typedef LAS const char* lds_cptr;
typedef LAS char* lds_ptr;
typedef __attribute__((address_space(1))) unsigned long long gu64;

constexpr int QO_OFF = 0;
constexpr int WO_OFF = 24576, WO_SLOT = 8192;
constexpr int Q1_OFF = 40960;
constexpr int KV_OFF = 65536, KV_SLOT = 16384, KV_END = 163840;
constexpr int KV_FIRST = KV_OFF + 4 * KV_SLOT;
constexpr int WS_A = 0, WS_C = 49152, WS_B = 81920;
constexpr int XS_0 = 32768, XS_1 = 114688;
constexpr int kvhalf(int tile, int v) { return KV_OFF + ((tile + 4) % 6) * KV_SLOT + 8192 * v; }
constexpr int TAILC(int i) {
  return i == 0 ? kvhalf(58, 0) : i == 1 ? kvhalf(58, 1) : i == 2 ? kvhalf(59, 0) : i == 3 ? kvhalf(60, 0) : i == 4 ? kvhalf(59, 1) : i == 5 ? kvhalf(61, 0)
       : i == 6 ? kvhalf(60, 1) : i == 7 ? kvhalf(62, 0) : i == 8 ? kvhalf(61, 1) : i == 9 ? kvhalf(63, 0)
       : i == 10 ? WO_OFF + 3 * WO_SLOT : i == 11 ? WO_OFF + 4 * WO_SLOT : i == 12 ? WO_OFF + 0 * WO_SLOT : i == 13 ? kvhalf(62, 1) : i == 14 ? WO_OFF + 1 * WO_SLOT : kvhalf(63, 1);
}
constexpr int RDY_OFF = WO_OFF + 2 * WO_SLOT;
constexpr int OWN_OFF = KV_OFF;
constexpr int R3B_OFF = 114688;
constexpr int LDS_BYTES = 163840;
constexpr size_t SLAB_BYTES = 98304;
constexpr size_t FLAG_OFF = 32u << 20;
constexpr float C_SCALE = 0.08838834764831845f * 1.4426950408889634f;
constexpr float THR = 6.0f;

constexpr unsigned STEP_AVG = 62, STEP_MIN = 46;
constexpr int AUX_SC1 = 16;

#define SBAR() __builtin_amdgcn_sched_barrier(0)
#define PIN(x) asm volatile("" : "+v"(x))
#define MFMA(a, b, c) __builtin_amdgcn_mfma_f32_32x32x16_bf16((a), (b), (c), 0, 0, 0)

DI unsigned cvtpk(float lo, float hi) { f32x2 v = {lo, hi}; bf16x2 b = __builtin_convertvector(v, bf16x2); return __builtin_bit_cast(unsigned, b); }
DI bf16x8 ld128(lds_cptr p) { return *(const LAS bf16x8*)p; }
DI void st128(lds_ptr p, u32x4 v) { *(LAS u32x4*)p = v; }
DI s16x4 ldtr(lds_cptr p) { typedef short v4i16_t __attribute__((ext_vector_type(4))); return __builtin_bit_cast(s16x4, __builtin_amdgcn_ds_read_tr16_b64_v4i16((LAS v4i16_t*)p)); }
DI bf16x8 cat(s16x4 lo, s16x4 hi) { return __builtin_shufflevector(lo, hi, 0, 1, 2, 3, 4, 5, 6, 7); }
DI bf16x8 ldtr2(lds_cptr p, int second) { return cat(ldtr(p), ldtr(p + second)); }
DI u32x4 pack8s(const f32x16& x, int s, float sc) {
  u32x4 w = {cvtpk(x[8 * s] * sc, x[8 * s + 1] * sc), cvtpk(x[8 * s + 2] * sc, x[8 * s + 3] * sc), cvtpk(x[8 * s + 4] * sc, x[8 * s + 5] * sc), cvtpk(x[8 * s + 6] * sc, x[8 * s + 7] * sc)};
  return w;
}
DI u32x4 pack8n(const f32x16& x, int s) {
  u32x4 w = {cvtpk(x[8 * s], x[8 * s + 1]), cvtpk(x[8 * s + 2], x[8 * s + 3]), cvtpk(x[8 * s + 4], x[8 * s + 5]), cvtpk(x[8 * s + 6], x[8 * s + 7])};
  return w;
}
DI bf16x8 asfrag(u32x4 v) { return __builtin_bit_cast(bf16x8, v); }
DI void dma16(__amdgpu_buffer_rsrc_t srd, unsigned voff, unsigned soff, unsigned ldsaddr) {
  asm volatile("s_mov_b32 m0, %0\n\ts_nop 3\n\tbuffer_load_dwordx4 %1, %2, %3 offen lds" ::"s"(ldsaddr), "v"(voff), "s"(srd), "s"(soff) : "memory");
}
DI void dma16nt(__amdgpu_buffer_rsrc_t srd, unsigned voff, unsigned soff, unsigned ldsaddr) {
  asm volatile("s_mov_b32 m0, %0\n\ts_nop 3\n\tbuffer_load_dwordx4 %1, %2, %3 offen nt lds" ::"s"(ldsaddr), "v"(voff), "s"(srd), "s"(soff) : "memory");
}
DI int fresh_lane() { int l; asm volatile("v_mbcnt_lo_u32_b32 %0, -1, 0\n\tv_mbcnt_hi_u32_b32 %0, -1, %0" : "=v"(l)); return l; }
#define LANE_CONSTS() const int lane = fresh_lane(); const int l32 = lane & 31, h = lane >> 5; const int blk = (lane >> 4) & 1, q4 = (lane & 15) >> 2, p = lane & 3; \
  const unsigned r8 = (lane >> 2) & 7, slot4 = lane & 3; (void)l32; (void)h; (void)blk; (void)q4; (void)p; (void)r8; (void)slot4
#define WAITVM(n) asm volatile("s_waitcnt vmcnt(" #n ")" ::: "memory")

#define BAR() do { __builtin_amdgcn_sched_barrier(0); __builtin_amdgcn_s_waitcnt(0xC07F); asm volatile("s_barrier" ::: "memory"); } while (0)

DI float rowmax16(const f32x16& c) {
  float a = fmaxf(fmaxf(c[0], c[1]), c[2]), b = fmaxf(fmaxf(c[3], c[4]), c[5]);
  a = fmaxf(fmaxf(a, c[6]), c[7]); b = fmaxf(fmaxf(b, c[8]), c[9]);
  a = fmaxf(fmaxf(a, c[10]), c[11]); b = fmaxf(fmaxf(b, c[12]), c[13]);
  a = fmaxf(fmaxf(a, c[14]), c[15]);
  const float m = fmaxf(a, b);
  auto rr = __builtin_amdgcn_permlane32_swap(__float_as_uint(m), __float_as_uint(m), false, false);
  return fmaxf(__uint_as_float(rr[0]), __uint_as_float(rr[1]));
}

__global__ __launch_bounds__(512, 2) void fused_attn(const u16* __restrict__ xA, const u16* __restrict__ kA, const u16* __restrict__ vA, const u16* __restrict__ wqA,
                                                   const u16* __restrict__ woA, const u16* __restrict__ xB, const u16* __restrict__ kB, const u16* __restrict__ vB,
                                                   const u16* __restrict__ wqB, const u16* __restrict__ woB, char* __restrict__ ws, u16* __restrict__ out
) {
  extern __shared__ __attribute__((aligned(16))) char smem[];
  const lds_ptr lds = (lds_ptr)smem;
  const unsigned lds0 = (unsigned)(uintptr_t)smem;
  const int w = __builtin_amdgcn_readfirstlane(threadIdx.x >> 6);
  const int bid = blockIdx.x, xcd = bid & 7, idx = bid >> 3;
  const int br = xcd >> 2, g = 2 * (xcd & 3) + (idx & 1), b = idx >> 1;
  const unsigned krot = (idx + 2 * xcd) & 15;
  gu64* const flagp = (gu64*)(ws + FLAG_OFF + (size_t)(br * 16 + b) * 256);
  unsigned gen_lo = 0, gen_hi = 0;

  auto qphase = [&](f32x16 (&acc)[4][3], int head) __attribute__((always_inline)) {
    LANE_CONSTS();
    const unsigned wqb = 8192u * head + 2048u * h + 64u * q4 + 32u * blk + 16u * (p & 1) + 8u * (p >> 1);
    const unsigned xb0 = 1024u * (l32 >> 3) + 64u * (l32 & 7) + 16u * (h ^ ((l32 >> 2) & 3));
#pragma unroll
    for (int d = 0; d < 4; ++d)
#pragma unroll
      for (int q = 0; q < 3; ++q) acc[d][q] = f32x16{};
    bf16x8 wa[2], wb[2], x0[3], x1[3];
    __builtin_amdgcn_s_setprio(3);
    BAR();
#define QM(W, X, D0, i) acc[D0 + (i) / 3][(i) % 3] = MFMA(W[(i) / 3], X[(i) % 3], acc[D0 + (i) / 3][(i) % 3])
#define QW(KS, D) ldtr2(wp + 4096 * (KS) + 512 * (D), 256)
#define QMM(W, X, D0) do { QM(W, X, D0, 0); QM(W, X, D0, 1); QM(W, X, D0, 2); QM(W, X, D0, 3); QM(W, X, D0, 4); QM(W, X, D0, 5); SBAR(); } while (0)
#define QSTEP(j, FIRST) do { \
      const lds_cptr wp = lds + st + wqb; \
      const int xs = (((j) & 2) ? XS_1 : XS_0) + 512 * ((j) & 1); \
      const lds_cptr xp0 = lds + xs + xb0, xp1 = lds + xs + (xb0 ^ 32u); \
      if (FIRST) { x0[0] = ld128(xp0); x0[1] = ld128(xp0 + 4096); x0[2] = ld128(xp0 + 8192); wa[0] = QW(0, 0); wa[1] = QW(0, 1); SBAR(); } \
      else { \
        QM(wb, x1, 2, 0); x0[0] = ld128(xp0); x0[1] = ld128(xp0 + 4096); SBAR(); \
        QM(wb, x1, 2, 1); x0[2] = ld128(xp0 + 8192); wa[0] = QW(0, 0); SBAR(); \
        QM(wb, x1, 2, 2); wa[1] = QW(0, 1); SBAR(); \
        QM(wb, x1, 2, 3); QM(wb, x1, 2, 4); QM(wb, x1, 2, 5); SBAR(); } \
      QM(wa, x0, 0, 0); wb[0] = QW(0, 2); SBAR(); \
      QM(wa, x0, 0, 1); wb[1] = QW(0, 3); SBAR(); \
      QM(wa, x0, 0, 2); x1[0] = ld128(xp1); x1[1] = ld128(xp1 + 4096); SBAR(); \
      QM(wa, x0, 0, 3); x1[2] = ld128(xp1 + 8192); SBAR(); \
      QM(wa, x0, 0, 4); QM(wa, x0, 0, 5); SBAR(); \
      QM(wb, x0, 2, 0); wa[0] = QW(1, 0); SBAR(); \
      QM(wb, x0, 2, 1); wa[1] = QW(1, 1); SBAR(); \
      QM(wb, x0, 2, 2); QM(wb, x0, 2, 3); QM(wb, x0, 2, 4); QM(wb, x0, 2, 5); SBAR(); \
      QM(wa, x1, 0, 0); wb[0] = QW(1, 2); SBAR(); \
      QM(wa, x1, 0, 1); wb[1] = QW(1, 3); SBAR(); \
      QM(wa, x1, 0, 2); QM(wa, x1, 0, 3); QM(wa, x1, 0, 4); QM(wa, x1, 0, 5); \
      st = (st == WS_A) ? WS_C : (st == WS_C) ? WS_B : WS_A; \
      BAR(); } while (0)
    int st = WS_A;
    QSTEP(0, true);
#pragma unroll 1
    for (int j = 1; j < 16; ++j) {
      QSTEP(j, false);
    }
#undef QSTEP
    QMM(wb, x1, 2);
#undef QMM
#undef QM
#undef QW
  };

  if (w < 3) {
    {
      f32x16 acc[4][3];
      qphase(acc, (w < 2) ? w : 3);
      const int lane = fresh_lane();
      const int o0 = (w == 0) ? QO_OFF : (w == 1) ? Q1_OFF : WO_OFF, o2 = (w == 0) ? QO_OFF + 16384 : (w == 1) ? Q1_OFF + 16384 : R3B_OFF;
#pragma unroll
      for (int q = 0; q < 3; ++q) {
        const lds_ptr dst = lds + ((q < 2) ? o0 + q * 8192 : o2) + lane * 16;
#pragma unroll
        for (int d = 0; d < 4; ++d) { st128(dst + (2 * d) * 1024, pack8s(acc[d][q], 0, C_SCALE)); st128(dst + (2 * d + 1) * 1024, pack8s(acc[d][q], 1, C_SCALE)); }
      }
      BAR();
    }
    {
      const int qb = w;
      LANE_CONSTS();

      const unsigned kb0 = 2048u * (l32 >> 3) + 64u * (l32 & 7) + 16u * (h ^ ((l32 >> 2) & 3));
      const unsigned vb0 = 8192u + 256u * h + 64u * q4 + 32u * blk + 8u * p;
      const lds_ptr qo = lds + QO_OFF + qb * 8192 + lane * 16;
      f32x16 o[4], C, P, negm;
      bf16x8 qf[8], kf[8], vf[8];
      u32x4 pw0, pw1;
      float lsum = 0.f;
      int kvp = KV_FIRST, kvc = KV_FIRST;
      auto nextslot = [](int s) { return (s == KV_END - KV_SLOT) ? KV_OFF : s + KV_SLOT; };
#define EX(v) __builtin_amdgcn_exp2f(v)
#define MX3(a, b, c) fmaxf(fmaxf((a), (b)), (c))
#define SETM(x) do { _Pragma("unroll") for (int r_ = 0; r_ < 16; ++r_) negm[r_] = (x); PIN(negm); } while (0)
#define KRD_(i) kf[i] = ld128(((i & 1) ? kp1 : kp0) + 512 * (i >> 1))
#define KRD(i) do { if (MODE != 1) KRD_(i); } while (0)
#define VRD(i) vf[i] = ldtr2(vp + 4096 * ((i) >> 2) + 512 * ((i) & 3), 2048)
      {
#pragma unroll
        for (int s = 0; s < 8; ++s) qf[s] = ld128(qo + s * 1024);
        { const lds_cptr kp0 = lds + kvc + kb0, kp1 = lds + kvc + (kb0 ^ 32u);
#pragma unroll
          for (int s = 0; s < 8; ++s) KRD_(s); }
        C = MFMA(kf[0], qf[0], f32x16{});
#pragma unroll
        for (int s = 1; s < 8; ++s) C = MFMA(kf[s], qf[s], C);
        SBAR();
        kvc = nextslot(kvc);
        { const lds_cptr kp0 = lds + kvc + kb0, kp1 = lds + kvc + (kb0 ^ 32u);
#pragma unroll
          for (int s = 0; s < 8; ++s) KRD_(s); }
        const float m0 = rowmax16(C);
        SETM(-m0);
#pragma unroll
        for (int r = 0; r < 16; ++r) P[r] = EX(C[r] - m0);
        BAR();
      }
#define GAPA(i) do { VRD(i); if (MODE != 2) { if (i == 0) C = MFMA(kf[0], qf[0], MODE == 0 ? negm : f32x16{}); else C = MFMA(kf[i], qf[i], C); } \
        if (i < 7) { sacc += P[2 * i + 2]; sacc += P[2 * i + 3]; } \
        if (i < 4) pw0[i] = cvtpk(P[2 * i], P[2 * i + 1]); else pw1[i - 4] = cvtpk(P[2 * i], P[2 * i + 1]); \
        PIN(sacc); if (i < 4) PIN(pw0); else PIN(pw1); SBAR(); } while (0)
#define PVM(i) o[(i) & 3] = MFMA(vf[i], asfrag(((i) < 4) ? pw0 : pw1), (ZC && (i) < 4) ? f32x16{} : o[(i) & 3])
#define QRD(i) do { if (QPF) qf[i] = ld128(qsrc + (i) * 1024); } while (0)
#define EP(r) P[r] = EX(C[r] - m0)
#define STEP(MODE_, ZC_, QPF_) do { constexpr int MODE = MODE_; constexpr bool ZC = ZC_, QPF = QPF_; \
        const lds_cptr vp = lds + kvp + vb0; \
        kvp = kvc; kvc = nextslot(kvc); \
        const lds_cptr kp0 = lds + kvc + kb0, kp1 = lds + kvc + (kb0 ^ 32u); \
        float sacc = P[0] + P[1]; \
        GAPA(0); GAPA(1); GAPA(2); GAPA(3); GAPA(4); GAPA(5); GAPA(6); GAPA(7); \
        lsum += sacc; \
        if (MODE == 0) { \
          constexpr float m0 = 0.f; float ma, mb; \
          PVM(0); KRD(0); KRD(1); QRD(0); SBAR(); \
          PVM(1); KRD(2); KRD(3); QRD(1); SBAR(); \
          PVM(2); KRD(4); QRD(2); EP(0); EP(1); PIN(P); SBAR(); \
          PVM(3); KRD(5); QRD(3); EP(2); EP(3); PIN(P); SBAR(); \
          PVM(4); KRD(6); QRD(4); EP(4); EP(5); PIN(P); SBAR(); \
          PVM(5); KRD(7); QRD(5); EP(6); EP(7); PIN(P); SBAR(); \
          PVM(6); QRD(6); EP(8); EP(9); ma = MX3(C[0], C[1], C[2]); PIN(P); PIN(ma); SBAR(); \
          PVM(7); QRD(7); EP(10); EP(11); mb = MX3(C[3], C[4], C[5]); PIN(P); PIN(mb); SBAR(); \
          EP(12); EP(13); EP(14); EP(15); \
          ma = MX3(ma, C[6], C[7]); mb = MX3(mb, C[8], C[9]); ma = MX3(ma, C[10], C[11]); mb = MX3(mb, C[12], C[13]); ma = MX3(ma, C[14], C[15]); PIN(P); \
          ma = fmaxf(ma, mb); \
          auto rr = __builtin_amdgcn_permlane32_swap(__float_as_uint(ma), __float_as_uint(ma), false, false); \
          const float rm = fmaxf(__uint_as_float(rr[0]), __uint_as_float(rr[1]));     \
          if (__builtin_expect(__any(rm > THR), 0)) { \
            const float dl = fmaxf(rm, 0.f), alpha = __builtin_amdgcn_exp2f(-dl), nm_ = negm[0] - dl; \
            lsum *= alpha; SETM(nm_); \
            _Pragma("unroll") for (int r = 0; r < 16; ++r) P[r] = EX(C[r] - dl); \
            _Pragma("unroll") for (int dt = 0; dt < 4; ++dt) _Pragma("unroll") for (int r = 0; r < 16; ++r) o[dt][r] *= alpha; \
          } \
        } else { \
          const float lold = lsum; float mnew = 0.f; \
          PVM(0); PVM(1); SBAR(); \
          if (MODE == 1) { \
            const float m0 = rowmax16(C); mnew = m0; lsum = 0.f; SBAR(); \
            PVM(2); EP(0); EP(1); EP(2); PIN(P); SBAR(); \
            PVM(3); EP(3); EP(4); EP(5); PIN(P); SBAR(); \
            PVM(4); EP(6); EP(7); EP(8); PIN(P); SBAR(); \
            PVM(5); EP(9); EP(10); EP(11); PIN(P); SBAR(); \
            PVM(6); EP(12); EP(13); PIN(P); SBAR(); \
            PVM(7); EP(14); EP(15); PIN(P); SBAR(); \
          } else { PVM(2); PVM(3); PVM(4); PVM(5); PVM(6); PVM(7); } \
            \
          auto rr = __builtin_amdgcn_permlane32_swap(__float_as_uint(lold), __float_as_uint(lold), false, false); \
          const float inv = __builtin_amdgcn_rcpf(__uint_as_float(rr[0]) + __uint_as_float(rr[1])); \
          _Pragma("unroll") for (int dt = 0; dt < 4; ++dt) { st128(qo + (2 * dt) * 1024, pack8s(o[dt], 0, inv)); st128(qo + (2 * dt + 1) * 1024, pack8s(o[dt], 1, inv)); \
            if (MODE == 1) { SBAR(); if (dt == 0) { KRD_(0); KRD_(1); KRD_(2); KRD_(3); } if (dt == 1) { KRD_(4); KRD_(5); KRD_(6); KRD_(7); } SBAR(); } } \
          if (MODE == 1) SETM(-mnew); \
        } \
        BAR(); } while (0)
      lds_cptr qsrc = lds + Q1_OFF + qb * 8192 + lane * 16;
#pragma unroll 1
      for (int u = 0; u < 4; ++u) {
        STEP(0, true, false);
#pragma unroll 1
        for (int t = 0; t < 13; ++t) STEP(0, false, false);
        STEP(0, false, true);
        qsrc = qo;
        if (u == 3) break;
        STEP(1, false, false);
      }
      STEP(2, false, false);
#undef STEP
#undef PVM
#undef QRD
#undef GAPA
#undef VRD
#undef KRD
#undef KRD_
#undef EX
#undef EP
#undef MX3
#undef SETM
      BAR();
    }
  } else if (w == 3) {
    bf16x8 q2[2][3][8];
    {
      f32x16 acc[4][3];
      qphase(acc, 2);
#pragma unroll
      for (int q = 0; q < 3; ++q)
#pragma unroll
        for (int d = 0; d < 4; ++d) { q2[0][q][2 * d] = asfrag(pack8s(acc[d][q], 0, C_SCALE)); q2[0][q][2 * d + 1] = asfrag(pack8s(acc[d][q], 1, C_SCALE)); }
      BAR();
    }
    {
      LANE_CONSTS();
      const u16* kg = br ? kB : kA; const u16* vg = br ? vB : vA; const u16* wog = br ? woB : woA;
      const size_t kvbase = ((size_t)b * 32 + 4 * g) * (512 * 128);
      const __amdgpu_buffer_rsrc_t srdK = __builtin_amdgcn_make_buffer_rsrc((void*)(kg + kvbase), 0, 4 * 512 * 128 * 2, 0x00020000);
      const __amdgpu_buffer_rsrc_t srdV = __builtin_amdgcn_make_buffer_rsrc((void*)(vg + kvbase), 0, 4 * 512 * 128 * 2, 0x00020000);
      const __amdgpu_buffer_rsrc_t srdWo = __builtin_amdgcn_make_buffer_rsrc((void*)(wog + (size_t)g * 512 * 512), 0, 512 * 512 * 2, 0x00020000);
      const unsigned voffV = r8 * 256u + 64u * h + 16u * slot4;
      const unsigned voffK0 = r8 * 256u + 64u * h + 16u * (slot4 ^ (r8 >> 2)), voffK1 = voffK0 ^ 32u;
      const unsigned voffWo = r8 * 1024u + 64u * h + 16u * slot4;
      auto issue_kv = [&](int T, int slot) {
        const unsigned so = (((unsigned)T & ~15u) | (((unsigned)T + krot) & 15u)) * 8192u, dst = lds0 + slot;
#pragma unroll
        for (int i = 0; i < 8; ++i) dma16nt(srdK, ((i >> 1) & 1) ? voffK1 : voffK0, so + 2048u * (i >> 1) + 128u * (i & 1), dst + 1024u * i);
#pragma unroll
        for (int i = 0; i < 8; ++i) dma16nt(srdV, voffV, so + 2048u * (i >> 1) + 128u * (i & 1), dst + 8192u + 1024u * i);
      };
      auto issue_woc = [&](int r, int a, int dstoff) {
        const unsigned rr = (r + (b >> 1)) & 7;
        const unsigned so = (384u + 16u * rr) * 1024u + ((a ^ b) & 1) * 512u, dst = lds0 + dstoff;
#pragma unroll
        for (int i = 0; i < 8; ++i) dma16(srdWo, voffWo, so + 8192u * (i >> 2) + 128u * (i & 3), dst + 1024u * i);
      };
      {
        const lds_cptr h0 = lds + WO_OFF + lane * 16, h2 = lds + R3B_OFF + lane * 16;
#pragma unroll
        for (int s = 0; s < 8; ++s) { q2[1][0][s] = ld128(h0 + s * 1024); q2[1][1][s] = ld128(h0 + 8192 + s * 1024); q2[1][2][s] = ld128(h2 + s * 1024); }
      }
      BAR();
      BAR();
      int islot = KV_FIRST;
      const lds_ptr qo = lds + QO_OFF + lane * 16;
      unsigned long long sched = __builtin_amdgcn_s_memrealtime();
      for (int T = 2; T < 60; ++T) {
        sched += STEP_AVG;
        const unsigned long long tfl = __builtin_amdgcn_s_memrealtime() + STEP_MIN;
        const unsigned long long tmin = sched > tfl ? sched : tfl;
        issue_kv(T + 4, islot);
        islot = (islot == KV_END - KV_SLOT) ? KV_OFF : islot + KV_SLOT;
        if (T == 30) {
#pragma unroll
          for (int q = 0; q < 3; ++q)
#pragma unroll
            for (int s = 0; s < 8; ++s) st128(qo + q * 8192 + s * 1024, __builtin_bit_cast(u32x4, q2[0][q][s]));
        }
        if (T == 46) {
#pragma unroll
          for (int q = 0; q < 3; ++q)
#pragma unroll
            for (int s = 0; s < 8; ++s) st128(qo + q * 8192 + s * 1024, __builtin_bit_cast(u32x4, q2[1][q][s]));
        }
        WAITVM(32);
        while (__builtin_amdgcn_s_memrealtime() < tmin) __builtin_amdgcn_s_sleep(1);
        BAR();
      }
      issue_woc(0, 0, TAILC(0)); issue_woc(1, 0, TAILC(1)); issue_woc(2, 0, TAILC(2)); issue_woc(3, 0, TAILC(3)); WAITVM(48); BAR();
      issue_woc(4, 0, TAILC(4)); issue_woc(5, 0, TAILC(5)); WAITVM(48); BAR();
      issue_woc(6, 0, TAILC(6)); issue_woc(7, 0, TAILC(7)); BAR();
      issue_woc(0, 1, TAILC(8)); issue_woc(1, 1, TAILC(9)); BAR();
      issue_woc(5, 1, TAILC(13)); WAITVM(0); BAR();
      issue_woc(7, 1, TAILC(15));
      if (lane == 0) { *(LAS unsigned*)(lds + RDY_OFF) = 0u; *(LAS unsigned*)(lds + RDY_OFF + 4) = 0u; }
      WAITVM(0);
      BAR();
    }
  } else {
    const int pw = w - 4;
    const u16* wog = br ? woB : woA;
    {
      LANE_CONSTS();
      const u16* xg = br ? xB : xA; const u16* wqg = br ? wqB : wqA; const u16* kg = br ? kB : kA; const u16* vg = br ? vB : vA;
      const size_t kvbase = ((size_t)b * 32 + 4 * g) * (512 * 128);
      const __amdgpu_buffer_rsrc_t srdWq = __builtin_amdgcn_make_buffer_rsrc((void*)(wqg + g * 512), 0, 512 * 4096 * 2 - g * 1024, 0x00020000);
      const __amdgpu_buffer_rsrc_t srdX = __builtin_amdgcn_make_buffer_rsrc((void*)(xg + (size_t)b * 96 * 512), 0, 96 * 512 * 2, 0x00020000);
      const __amdgpu_buffer_rsrc_t srdK = __builtin_amdgcn_make_buffer_rsrc((void*)(kg + kvbase), 0, 4 * 512 * 128 * 2, 0x00020000);
      const __amdgpu_buffer_rsrc_t srdV = __builtin_amdgcn_make_buffer_rsrc((void*)(vg + kvbase), 0, 4 * 512 * 128 * 2, 0x00020000);
      const unsigned voffWq = r8 * 8192u + 64u * h + 16u * slot4;
      const unsigned voffX0 = r8 * 1024u + 64u * h + 16u * (slot4 ^ (r8 >> 2)), voffX1 = voffX0 ^ 32u;
      const unsigned voffK = (8u * pw + r8) * 256u + 64u * h + 16u * (slot4 ^ (r8 >> 2) ^ (2u * (pw & 1)));
      const unsigned voffV = (8u * pw + r8) * 256u + 64u * h + 16u * slot4;
      const unsigned rot = 2u * (b & 7);
      auto issue_w = [&](int j, int st) {
        const unsigned jj = (j + rot) & 15;
        const unsigned so = jj * (32u * 8192u) + pw * 256u, dst = lds0 + st + 8192u * pw;
#pragma unroll
        for (int i = 0; i < 8; ++i) dma16nt(srdWq, voffWq, so + 65536u * (i >> 1) + 128u * (i & 1), dst + 1024u * i);
      };
      auto issue_x = [&](int j) {
        const unsigned jj = (j + rot) & 15;
        const unsigned dst = lds0 + ((j & 2) ? XS_1 : XS_0) + 3072u * pw;
#pragma unroll
        for (int i = 0; i < 3; ++i) dma16nt(srdX, ((3 * pw + i) & 1) ? voffX1 : voffX0, jj * 64u + (3u * pw + i) * 8192u, dst + 1024u * i);
      };
      auto issue_t = [&](int T) {
        const unsigned so = (((unsigned)T + krot) & 15u) * 8192u, dst = lds0 + KV_OFF + ((T + 4) % 6) * KV_SLOT + 2048u * pw;
        dma16nt(srdK, voffK, so, dst); dma16nt(srdK, voffK, so + 128u, dst + 1024u);
        dma16nt(srdV, voffV, so, dst + 8192u); dma16nt(srdV, voffV, so + 128u, dst + 8192u + 1024u);
      };
      auto nextst = [](int st) { return (st == WS_A) ? WS_C : (st == WS_C) ? WS_B : WS_A; };
#define BARN() asm volatile("s_barrier" ::: "memory")
      issue_x(0);
      issue_w(0, WS_A);
      issue_w(1, WS_C);
      WAITVM(8);
      BARN();
      int st2 = WS_B;
#define LEVEN(j) do { issue_x((j) + 2); issue_w((j) + 2, st2); st2 = nextst(st2); WAITVM(11); BARN(); } while (0)
#define LODD(j) do { issue_w((j) + 2, st2); st2 = nextst(st2); WAITVM(8); BARN(); } while (0)
#define LODDT(j, T) do { issue_w((j) + 2, st2); st2 = nextst(st2); issue_t(T); WAITVM(12); BARN(); } while (0)
      LEVEN(0); LODD(1); LEVEN(2); LODD(3); LEVEN(4); LODDT(5, 0); LEVEN(6); LODD(7); LEVEN(8); LODDT(9, 1); LEVEN(10); LODD(11); LEVEN(12);
      issue_w(15, st2); WAITVM(8); BARN();
#undef LEVEN
#undef LODD
#undef LODDT
#undef BARN
      issue_t(2); WAITVM(4); BAR();
      issue_t(3); issue_t(4); WAITVM(12); BAR();
      BAR();
      WAITVM(8); BAR();
      issue_t(5); WAITVM(8); BAR();
      WAITVM(4); BAR();
      WAITVM(0); BAR();
      if (pw == 3) { const unsigned long long g0 = __hip_atomic_load(flagp, __ATOMIC_RELAXED, __HIP_MEMORY_SCOPE_AGENT);
        gen_lo = __builtin_amdgcn_readfirstlane((unsigned)g0); gen_hi = __builtin_amdgcn_readfirstlane((unsigned)(g0 >> 32)); }
    }
    LANE_CONSTS();
    const __amdgpu_buffer_rsrc_t srdWo = __builtin_amdgcn_make_buffer_rsrc((void*)(wog + (size_t)g * 512 * 512), 0, 512 * 512 * 2, 0x00020000);
    const unsigned voffWo = (8u * (pw >> 1) + r8) * 1024u + 64u * (4u * (pw & 1) + h) + 16u * slot4;
    auto issue_wo = [&](int u, int m, int slot) {
      const unsigned rr = ((m >> 1) + (b >> 1)) & 7;
      const unsigned so = (128u * u + 16u * rr) * 1024u + ((m ^ b) & 1) * 512u;
      const unsigned dst = lds0 + WO_OFF + slot + 2048u * pw;
      dma16(srdWo, voffWo, so, dst);
      dma16(srdWo, voffWo, so + 128u, dst + 1024u);
    };
    const unsigned ob0 = 256u * h + 64u * q4 + 32u * blk + 16u * (p & 1) + 8u * (p >> 1) + 1024u * pw;
    const lds_cptr qo = lds + QO_OFF + lane * 16;
    f32x16 acc[2][2][3];
#pragma unroll
    for (int a = 0; a < 2; ++a)
#pragma unroll
      for (int c = 0; c < 2; ++c)
#pragma unroll
        for (int qb = 0; qb < 3; ++qb) acc[a][c][qb] = f32x16{};
    bf16x8 of[3], ofn[3];
    auto issue_M = [&](int M, int slot) { if (M < 48) issue_wo(M >> 4, M & 15, slot); else issue_wo(3, (M == 48) ? 5 : (M == 49) ? 7 : (M == 50) ? 9 : 13, slot); };
    issue_wo(0, 0, 0 * WO_SLOT); issue_wo(0, 1, 1 * WO_SLOT);
    for (int T = 4; T < 16; ++T) BAR();
    issue_wo(0, 2, 2 * WO_SLOT); issue_wo(0, 3, 3 * WO_SLOT);
    WAITVM(6); BAR();
    int cur = 0, nxt = 4 * WO_SLOT;
    auto rot = [&]() { cur = (cur == 4 * WO_SLOT) ? 0 : cur + WO_SLOT; nxt = (nxt == 4 * WO_SLOT) ? 0 : nxt + WO_SLOT; };
    const int rb = b >> 1;
    for (int M = 0; M < 48; M += 2) {
      const int m = M & 15;
      {
        issue_M(M + 4, nxt);
        if (m == 0) {
#pragma unroll
          for (int qb = 0; qb < 3; ++qb) of[qb] = ld128(qo + qb * 8192 + (rb & 7) * 1024);
        }
        if (m != 14) {
#pragma unroll
          for (int qb = 0; qb < 3; ++qb) ofn[qb] = ld128(qo + qb * 8192 + (((m >> 1) + 1 + rb) & 7) * 1024);
        }
        const lds_cptr wp = lds + WO_OFF + cur + ob0;
#pragma unroll
        for (int c = 0; c < 2; ++c) {
          bf16x8 wf = ldtr2(wp + 512 * c, 4096);
#pragma unroll
          for (int qb = 0; qb < 3; ++qb) acc[0][c][qb] = MFMA(wf, of[qb], acc[0][c][qb]);
        }
        WAITVM(6);
        BAR();
        rot();
      }
      {
        issue_M(M + 5, nxt);
        const lds_cptr wp = lds + WO_OFF + cur + ob0;
#pragma unroll
        for (int c = 0; c < 2; ++c) {
          bf16x8 wf = ldtr2(wp + 512 * c, 4096);
#pragma unroll
          for (int qb = 0; qb < 3; ++qb) acc[1][c][qb] = MFMA(wf, of[qb], acc[1][c][qb]);
        }
#pragma unroll
        for (int qb = 0; qb < 3; ++qb) of[qb] = ofn[qb];
        if (M < 46) WAITVM(6); else WAITVM(0);
        BAR();
        rot();
      }
    }
    const __amdgpu_buffer_rsrc_t srdS = __builtin_amdgcn_make_buffer_rsrc((void*)(ws + (size_t)((br * 16 + b) * 8 + g) * SLAB_BYTES), 0, (int)SLAB_BYTES, 0x00020000);
    {
      bf16x8 wf[2][2], ot[2][3];
      auto rd = [&](int i, int s) __attribute__((always_inline)) {
        const lds_cptr wp = lds + TAILC(i) + ob0;
        wf[s][0] = ldtr2(wp, 4096); wf[s][1] = ldtr2(wp + 512, 4096);
#pragma unroll
        for (int qb = 0; qb < 3; ++qb) ot[s][qb] = ld128(qo + qb * 8192 + (((i & 7) + rb) & 7) * 1024);
      };
      auto store_half = [&](int a) __attribute__((always_inline)) {
#pragma unroll
        for (int qb = 0; qb < 3; ++qb)
#pragma unroll
          for (int c = 0; c < 2; ++c)
#pragma unroll
            for (int s = 0; s < 2; ++s)
              __builtin_amdgcn_raw_buffer_store_b128(pack8n(acc[a][c][qb], s), srdS, lane * 16, (4 * (a ^ (b & 1)) + pw) * 12288 + ((qb * 2 + c) * 2 + s) * 1024, AUX_SC1);
      };
      const int own_a = (g >> 2) ^ (b & 1);
      rd(0, 0);
#pragma unroll
      for (int i = 0; i < 16; ++i) {
        if (i == 14) BAR();
        if (i + 1 < 16) rd(i + 1, (i + 1) & 1);
        SBAR();
#pragma unroll
        for (int c = 0; c < 2; ++c)
#pragma unroll
          for (int qb = 0; qb < 3; ++qb) acc[i >> 3][c][qb] = MFMA(wf[i & 1][c], ot[i & 1][qb], acc[i >> 3][c][qb]);
        SBAR();
        if (i == 7 && !(pw == (g & 3) && own_a == 0)) store_half(0);
      }
      if (!(pw == (g & 3) && own_a == 1)) store_half(1);
      asm volatile("s_waitcnt vmcnt(0)" ::: "memory");
      asm volatile("s_waitcnt lgkmcnt(0)\n\ts_barrier" ::: "memory");
      if (pw == (g & 3)) {
        const lds_ptr op = lds + OWN_OFF + lane * 16;
#pragma unroll
        for (int qb = 0; qb < 3; ++qb)
#pragma unroll
          for (int c = 0; c < 2; ++c)
#pragma unroll
            for (int s = 0; s < 2; ++s) st128(op + ((qb * 2 + c) * 2 + s) * 1024, own_a ? pack8n(acc[1][c][qb], s) : pack8n(acc[0][c][qb], s));
        asm volatile("s_waitcnt lgkmcnt(0)" ::: "memory");
        if (lane == 0) *(volatile LAS unsigned*)(lds + RDY_OFF + 4) = 1u;
      }
    }
  }
  if (w < 4) asm volatile("s_waitcnt lgkmcnt(0)\n\ts_barrier" ::: "memory");
  LANE_CONSTS();
  volatile LAS unsigned* rdy = (volatile LAS unsigned*)(lds + RDY_OFF);
  if (w == 7) {
    const unsigned long long want = (((unsigned long long)gen_hi << 32) | gen_lo) + 1;
    if (lane == 0) __hip_atomic_store(flagp + 16 + g, want, __ATOMIC_RELAXED, __HIP_MEMORY_SCOPE_AGENT);
    unsigned m = 0;
    for (unsigned spins = 0; spins < (1u << 16); ++spins) {
      const unsigned long long f = __hip_atomic_load(flagp + 16 + (lane & 7), __ATOMIC_RELAXED, __HIP_MEMORY_SCOPE_AGENT);
      m = (unsigned)__ballot(f == want) & 0xffu;
      if (lane == 0) *rdy = m;
      if (m == 0xffu) break;
      __builtin_amdgcn_s_sleep(1);
    }
    if (lane == 0) *rdy = 0xffu;
    if (g == 0 && lane == 0) __hip_atomic_store(flagp, want, __ATOMIC_RELAXED, __HIP_MEMORY_SCOPE_AGENT);
  } else {
    const __amdgpu_buffer_rsrc_t srdG = __builtin_amdgcn_make_buffer_rsrc((void*)(ws + (size_t)((br * 16 + b) * 8) * SLAB_BYTES), 0, (int)(8 * SLAB_BYTES), 0x00020000);
    const bool two = w < 5;
    u32x4 v[2][8];
    unsigned done = 1;
#pragma unroll 1
    for (int pass = 0; pass < (1 << 22); ++pass) {
      const unsigned m = __builtin_amdgcn_readfirstlane(*rdy);
      __builtin_amdgcn_fence(__ATOMIC_ACQUIRE, "wavefront");
#pragma unroll
      for (int i = 1; i < 8; ++i) {
        const int s = (g + i) & 7;
        if (((m >> s) & 1u) && !((done >> i) & 1u)) {
          const int so = s * (int)SLAB_BYTES + g * 12288 + w * 1024;
          v[0][i] = __builtin_amdgcn_raw_buffer_load_b128(srdG, lane * 16, so, AUX_SC1);
          if (two) v[1][i] = __builtin_amdgcn_raw_buffer_load_b128(srdG, lane * 16, so + 7 * 1024, AUX_SC1);
          done |= 1u << i;
        }
      }
      if (done == 0xffu) break;
      __builtin_amdgcn_s_sleep(1);
    }
    while (__builtin_amdgcn_readfirstlane(rdy[1]) == 0u) __builtin_amdgcn_s_sleep(1);
    v[0][0] = *(const LAS u32x4*)(lds + OWN_OFF + w * 1024 + lane * 16);
    if (two) v[1][0] = *(const LAS u32x4*)(lds + OWN_OFF + (w + 7) * 1024 + lane * 16);
#pragma unroll
    for (int i = 0; i < 2; ++i)
      if (i == 0 || two) {
        float a[8] = {0.f, 0.f, 0.f, 0.f, 0.f, 0.f, 0.f, 0.f};
#pragma unroll
        for (int s = 0; s < 8; ++s)
#pragma unroll
          for (int j = 0; j < 4; ++j) { a[2 * j] += __uint_as_float(v[i][s][j] << 16); a[2 * j + 1] += __uint_as_float(v[i][s][j] & 0xffff0000u); }
        const u32x4 r = {cvtpk(a[0], a[1]), cvtpk(a[2], a[3]), cvtpk(a[4], a[5]), cvtpk(a[6], a[7])};
        const int f = w + 7 * i, qb = f >> 2, c = (f >> 1) & 1, s = f & 1;
        const int q = 32 * qb + l32, n = 64 * g + 32 * c + 16 * s + 8 * h;
        *(u32x4*)(out + ((size_t)(b * 96 + q)) * 1024 + br * 512 + n) = r;
      }
  }
}

extern "C" void kernel_launch(void* const* d_in, const int* in_sizes, int n_in, void* d_out, int out_size, void* d_ws, size_t ws_size, hipStream_t stream) {
  (void)in_sizes; (void)n_in; (void)out_size; (void)ws_size;
  (void)hipFuncSetAttribute((const void*)fused_attn, hipFuncAttributeMaxDynamicSharedMemorySize, LDS_BYTES);
  hipLaunchKernelGGL(fused_attn, dim3(256), dim3(512), LDS_BYTES, stream,
                     (const u16*)d_in[0], (const u16*)d_in[1], (const u16*)d_in[2], (const u16*)d_in[3], (const u16*)d_in[4],
                     (const u16*)d_in[5], (const u16*)d_in[6], (const u16*)d_in[7], (const u16*)d_in[8], (const u16*)d_in[9], (char*)d_ws, (u16*)d_out
  );
}
```
